```python
import torch

def _softmax(x, axis=-1):
    return torch.softmax(x, dim=axis)
def _gelu(x):
    return torch.nn.functional.gelu(x, approximate="tanh")
def _silu(x):
    return torch.nn.functional.silu(x)
def _sigmoid(x):
    return torch.sigmoid(x)
def _layernorm(x, axis=-1, eps=1e-5):
    m = x.mean(dim=axis, keepdim=True)
    v = x.var(dim=axis, keepdim=True, unbiased=False)
    return (x - m) / torch.sqrt(v + eps)
def _scatter_add(tgt, idx, src, axis=0):
    return tgt.clone().index_add_(axis, idx, src)
def _segment_sum(src, seg, n):
    out = torch.zeros((n,) + src.shape[1:], dtype=src.dtype)
    return out.index_add_(0, seg, src)
def _pad(x, pw):
    flat = tuple(v for pair in reversed(pw) for v in pair)
    return torch.nn.functional.pad(x, flat)

kernel_name = 'parallel_sum__mha_block_cached__geglu_ffn__decode'

def setup_inputs():

    _w = (lambda a: (lambda t: t.to(torch.bfloat16) if t.is_floating_point() else t)(torch.as_tensor(a)))
    def _A_setup():
        B, Sq, D = 4, 1, 448
        H, Sk, Dh = 16, 4096, 256
        return {
            "x": _w((lambda *s: torch.randn(*s, dtype=torch.bfloat16) * 0.05)(B, Sq, D)),
            "k_cache": _w((lambda *s: torch.randn(*s, dtype=torch.bfloat16) * 3.2)(B, H, Sk, Dh)),
            "v_cache": _w((lambda *s: torch.randn(*s, dtype=torch.bfloat16) * 1.6)(B, H, Sk, Dh)),
            "wq": _w((lambda *s: torch.randn(*s, dtype=torch.bfloat16) * 3.2)(D, H * Dh) * (D ** -0.5)),
            "wo": _w((lambda *s: torch.randn(*s, dtype=torch.bfloat16) * 1.6)(H * Dh, D) * ((H * Dh) ** -0.5)),
        }
    _A_in = _A_setup()

    def _B_setup():
        B, S, D, F = 4, 1, 448, 1792
        return {
            "x": _w((lambda *s: torch.randn(*s, dtype=torch.bfloat16) * 0.05)(B, S, D)),
            "w_gate": _w((lambda *s: torch.randn(*s, dtype=torch.bfloat16) * 0.05)(D, F)),
            "w_up": _w((lambda *s: torch.randn(*s, dtype=torch.bfloat16) * 0.05)(D, F)),
            "w_down": _w((lambda *s: torch.randn(*s, dtype=torch.bfloat16) * 0.05)(F, D)),
        }
    _B_in = _B_setup()

    return {
        "A_x": _A_in["x"],
        "A_k_cache": _A_in["k_cache"],
        "A_v_cache": _A_in["v_cache"],
        "A_wq": _A_in["wq"],
        "A_wo": _A_in["wo"],
        "B_x": _B_in["x"],
        "B_w_gate": _B_in["w_gate"],
        "B_w_up": _B_in["w_up"],
        "B_w_down": _B_in["w_down"],
    }

def reference(A_x, A_k_cache, A_v_cache, A_wq, A_wo, B_x, B_w_gate, B_w_up, B_w_down):
    def _A_ref(x, k_cache, v_cache, wq, wo):
        B, Sq, D = x.shape
        H, Dh = 16, 256
        q = (lambda x, *d: x.permute(*d))((x @ wq).reshape(B, Sq, H, Dh), 0, 2, 1, 3)
        scores = torch.einsum("bhqd,bhkd->bhqk", q, k_cache) * (Dh ** -0.5)
        p = _softmax(scores, axis=-1)
        o = torch.einsum("bhqk,bhkd->bhqd", p, v_cache)
        return (lambda x, *d: x.permute(*d))(o, 0, 2, 1, 3).reshape(B, Sq, H * Dh) @ wo

    def _B_ref(x, w_gate, w_up, w_down):
        return (_gelu(x @ w_gate) * (x @ w_up)) @ w_down

    x = A_x
    xn = _layernorm(x, axis=-1)
    return x + _A_ref(xn, A_k_cache, A_v_cache, A_wq, A_wo) + _B_ref(xn, B_w_gate, B_w_up, B_w_down)
```

```cpp
#include <hip/hip_runtime.h>
#include <hip/hip_bf16.h>
#include <stdint.h>

#define MIMIC 0

namespace {
constexpr int B = 4, D = 448, H = 16, SK = 4096, DH = 256, F = 1792, HD = H * DH;
constexpr int NSPLIT = 4, KPW = 128;
using bf16x2 = __attribute__((ext_vector_type(2))) __bf16;
using u32x4 = __attribute__((ext_vector_type(4))) unsigned;
using f32x4 = __attribute__((ext_vector_type(4))) float;

__device__ __forceinline__ float bf2f(unsigned short u) { return __uint_as_float(((unsigned)u) << 16); }
__device__ __forceinline__ float lo_f(unsigned w) { return __uint_as_float(w << 16); }
__device__ __forceinline__ float hi_f(unsigned w) { return __uint_as_float(w & 0xffff0000u); }
__device__ __forceinline__ unsigned cvtpk(float lo, float hi) {
  unsigned r; asm volatile("v_cvt_pk_bf16_f32 %0, %1, %2" : "=v"(r) : "v"(lo), "v"(hi)); return r;
}
__device__ __forceinline__ float rbf(float x) {
  return x;
}
__device__ __forceinline__ float dot2(unsigned k, unsigned q, float acc) {
  return __builtin_amdgcn_fdot2_f32_bf16(__builtin_bit_cast(bf16x2, k), __builtin_bit_cast(bf16x2, q), acc, false);
}
__device__ __forceinline__ float wave_sum(float v) {
  for (int o = 32; o > 0; o >>= 1) v += __shfl_xor(v, o, 64);
  return v;
}
__device__ __forceinline__ float wave_max(float v) {
  for (int o = 32; o > 0; o >>= 1) v = fmaxf(v, __shfl_xor(v, o, 64));
  return v;
}

__device__ __forceinline__ void ln_row(const unsigned short* x, float* xn, int lane) {
  float v[7]; float s = 0.f;
  for (int i = 0; i < 7; ++i) { v[i] = bf2f(x[lane + 64 * i]); s += v[i]; }
  s = wave_sum(s);
  const float mean = s * (1.f / D);
  float q = 0.f;
  for (int i = 0; i < 7; ++i) { const float d = v[i] - mean; q += d * d; }
  q = wave_sum(q);
  const float var = rbf(q * (1.f / D));
  const float den = rbf(sqrtf(rbf(var + 1e-5f)));
  const float mb = rbf(mean);
  for (int i = 0; i < 7; ++i) xn[lane + 64 * i] = rbf(rbf(v[i] - mb) / den);
}

__global__ void __launch_bounds__(256) k_prep(const unsigned short* x, const unsigned short* wq, const unsigned short* wg,
                                              const unsigned short* wu, unsigned short* qout, float* hout) {
  __shared__ float xn[B][D];
  const int t = threadIdx.x, lane = t & 63, w = t >> 6;
  ln_row(x + w * D, xn[w], lane);
  __syncthreads();
  const int blk = blockIdx.x;
  if (blk < 16) {
    const int col = blk * 256 + t;
    float a[B] = {0, 0, 0, 0};
    for (int d = 0; d < D; ++d) { const float wv = bf2f(wq[(size_t)d * HD + col]);
      for (int b = 0; b < B; ++b) a[b] = fmaf(xn[b][d], wv, a[b]); }
    for (int b = 0; b < B; ++b) qout[b * HD + col] = (unsigned short)(cvtpk(a[b], a[b]) & 0xffffu);
  } else {
    const int col = (blk - 16) * 256 + t;
    float g[B] = {0, 0, 0, 0}, u[B] = {0, 0, 0, 0};
    for (int d = 0; d < D; ++d) { const float gv = bf2f(wg[(size_t)d * F + col]), uv = bf2f(wu[(size_t)d * F + col]);
      for (int b = 0; b < B; ++b) { g[b] = fmaf(xn[b][d], gv, g[b]); u[b] = fmaf(xn[b][d], uv, u[b]); } }
    for (int b = 0; b < B; ++b) {
      const float gg = rbf(g[b]), uu = rbf(u[b]);
      const float inner = 0.7978845608028654f * (gg + 0.044715f * gg * gg * gg);
      const float ge = rbf(0.5f * gg * (1.f + tanhf(inner)));
      hout[b * F + col] = rbf(ge * uu);
    }
  }
}

constexpr int W = 16;
__global__ void __launch_bounds__(512, 2) k_attn(const unsigned short* q, const unsigned short* K, const unsigned short* V,
                                                 float* po, float* pml) {
  __shared__ float sp[8][KPW];
  __shared__ float so[8][DH];
  __shared__ float sml[8][2];
  const int t = threadIdx.x, lane = t & 63, w = __builtin_amdgcn_readfirstlane(t >> 6), c = lane & 31, hf = lane >> 5;
  const int blk = blockIdx.x, bh = blk >> 2, s = blk & 3;
  const size_t base = ((size_t)bh * SK + s * 1024 + w * KPW) * DH;
  const u32x4* kp = (const u32x4*)(K + base) + lane;
  const u32x4* vp = (const u32x4*)(V + base) + lane;
  const u32x4 qv = *((const u32x4*)(q + bh * DH) + c);
  u32x4 buf[W];
#pragma unroll
  for (int i = 0; i < W; ++i) buf[i] = __builtin_nontemporal_load(kp + i * 64);
  float sc[64];
#pragma unroll
  for (int i = 0; i < 64; ++i) {
    const u32x4 k = buf[i % W];
    if (i + W < 64) buf[i % W] = __builtin_nontemporal_load(kp + (i + W) * 64);
    else buf[i % W] = __builtin_nontemporal_load(vp + (i + W - 64) * 64);
    float d = dot2(k[0], qv[0], 0.f); d = dot2(k[1], qv[1], d); d = dot2(k[2], qv[2], d); d = dot2(k[3], qv[3], d);
    for (int o = 16; o > 0; o >>= 1) d += __shfl_xor(d, o, 64);
    if (c == (i & 31)) sc[i >> 5] = d;
  }
  float s0 = rbf(sc[0]) * 0.0625f, s1 = rbf(sc[1]) * 0.0625f;
  const float m = wave_max(fmaxf(s0, s1));
  const float p0 = __expf(s0 - m), p1 = __expf(s1 - m);
  const float l = wave_sum(p0 + p1);
  sp[w][hf * 64 + c] = p0; sp[w][hf * 64 + 32 + c] = p1;
  float acc[8] = {0, 0, 0, 0, 0, 0, 0, 0};
  __builtin_amdgcn_wave_barrier();
#pragma unroll
  for (int i = 0; i < 64; ++i) {
    const u32x4 v = buf[i % W];
    if (i + W < 64) buf[i % W] = __builtin_nontemporal_load(vp + (i + W) * 64);
    const float p = sp[w][hf * 64 + i];
#pragma unroll
    for (int k = 0; k < 4; ++k) { acc[2 * k] = fmaf(p, lo_f(v[k]), acc[2 * k]); acc[2 * k + 1] = fmaf(p, hi_f(v[k]), acc[2 * k + 1]); }
  }
#pragma unroll
  for (int k = 0; k < 8; ++k) acc[k] += __shfl_xor(acc[k], 32, 64);
  if (hf == 0) { for (int k = 0; k < 8; ++k) so[w][8 * c + k] = acc[k]; }
  if (lane == 0) { sml[w][0] = m; sml[w][1] = l; }
  __syncthreads();
  if (t < DH) {
    float M = -INFINITY;
    for (int i = 0; i < 8; ++i) M = fmaxf(M, sml[i][0]);
    float L = 0.f, o = 0.f;
    for (int i = 0; i < 8; ++i) { const float wt = __expf(sml[i][0] - M); L = fmaf(wt, sml[i][1], L); o = fmaf(wt, so[i][t], o); }
    po[(size_t)blk * DH + t] = o;
    if (t == 0) { pml[blk * 2] = M; pml[blk * 2 + 1] = L; }
  }
}

__global__ void __launch_bounds__(1024) k_final(const unsigned short* x, const float* po, const float* pml, const float* hbuf,
                                                const unsigned short* wo, const unsigned short* wd, __hip_bfloat16* out) {
  __shared__ float o[HD];
  __shared__ float red[16][64], red2[16][64];
  const int t = threadIdx.x, b = blockIdx.x / 7, cb = blockIdx.x % 7, col = cb * 64 + (t & 63), g = t >> 6;
  for (int i = t; i < HD; i += 1024) {
    const int h = i >> 8, d = i & 255, bh = b * H + h;
    float M = -INFINITY;
    for (int s = 0; s < 4; ++s) M = fmaxf(M, pml[(bh * 4 + s) * 2]);
    float L = 0.f, a = 0.f;
    for (int s = 0; s < 4; ++s) { const float wt = __expf(pml[(bh * 4 + s) * 2] - M); L = fmaf(wt, pml[(bh * 4 + s) * 2 + 1], L);
      a = fmaf(wt, po[(size_t)(bh * 4 + s) * DH + d], a); }
    o[i] = rbf(a / L);
  }
  __syncthreads();
  float a = 0.f;
  for (int d = 0; d < DH; ++d) a = fmaf(o[g * DH + d], bf2f(wo[(size_t)(g * DH + d) * D + col]), a);
  float f = 0.f;
  for (int j = g * 112; j < (g + 1) * 112; ++j) f = fmaf(hbuf[b * F + j], bf2f(wd[(size_t)j * D + col]), f);
  red[g][t & 63] = a; red2[g][t & 63] = f;
  __syncthreads();
  if (t < 64) {
    float A = 0.f, Fv = 0.f;
    for (int i = 0; i < 16; ++i) { A += red[i][t]; Fv += red2[i][t]; }
    const float r = rbf(bf2f(x[b * D + col]) + rbf(A)) + rbf(Fv);
    ((unsigned short*)out)[b * D + col] = (unsigned short)(cvtpk(r, r) & 0xffffu);
  }
}
}

extern "C" void kernel_launch(void* const* d_in, const int* in_sizes, int n_in, void* d_out, int out_size, void* d_ws,
                              size_t ws_size, hipStream_t stream) {
  const unsigned short* x = (const unsigned short*)d_in[0];
  const unsigned short* K = (const unsigned short*)d_in[1];
  const unsigned short* V = (const unsigned short*)d_in[2];
  const unsigned short* wq = (const unsigned short*)d_in[3];
  const unsigned short* wo = (const unsigned short*)d_in[4];
  const unsigned short* wg = (const unsigned short*)d_in[6];
  const unsigned short* wu = (const unsigned short*)d_in[7];
  const unsigned short* wd = (const unsigned short*)d_in[8];
  char* ws = (char*)d_ws;
  unsigned short* q = (unsigned short*)ws;
  float* hbuf = (float*)(ws + 65536);
  float* pml = (float*)(ws + 131072);
  float* po = (float*)(ws + 262144);
  k_prep<<<23, 256, 0, stream>>>(x, wq, wg, wu, q, hbuf);
  k_attn<<<256, 512, 0, stream>>>(q, K, V, po, pml);
  k_final<<<28, 1024, 0, stream>>>(x, po, pml, hbuf, wo, wd, (__hip_bfloat16*)d_out);
}
```

```cpp
#include <hip/hip_runtime.h>
#include <hip/hip_bf16.h>
#include <stdint.h>

#define ROTM 0

namespace {
constexpr int D = 448, NH = 16, SK = 4096, DH = 256, F = 1792, HD = NH * DH;
constexpr int NCHUNK = 56;
typedef unsigned u32x4 __attribute__((ext_vector_type(4)));
typedef unsigned u32x2 __attribute__((ext_vector_type(2)));
typedef float f32x4 __attribute__((ext_vector_type(4)));
typedef float f32x2 __attribute__((ext_vector_type(2)));
typedef __bf16 bf16x2 __attribute__((ext_vector_type(2)));
typedef __amdgpu_buffer_rsrc_t rsrc_t;

__device__ __forceinline__ rsrc_t mk_rsrc(const void* p, unsigned bytes) {
  return __builtin_amdgcn_make_buffer_rsrc((void*)p, 0, bytes, 0x00020000);
}
__device__ __forceinline__ u32x4 ld_nt(rsrc_t r, int voff, int soff) { return __builtin_amdgcn_raw_buffer_load_b128(r, voff, soff, 2); }
__device__ __forceinline__ u32x4 ld_c(rsrc_t r, int voff, int soff) { return __builtin_amdgcn_raw_buffer_load_b128(r, voff, soff, 0); }
__device__ __forceinline__ float lo_f(unsigned w) { return __uint_as_float(w << 16); }
__device__ __forceinline__ float hi_f(unsigned w) { return __uint_as_float(w & 0xffff0000u); }
__device__ __forceinline__ unsigned cvtpk(float lo, float hi) {
  const bf16x2 r = {(__bf16)lo, (__bf16)hi}; return __builtin_bit_cast(unsigned, r);
}
__device__ __forceinline__ float rbf(float x) { return lo_f(cvtpk(x, x)); }
__device__ __forceinline__ float dot2(unsigned a, unsigned b, float acc) {
  return __builtin_amdgcn_fdot2_f32_bf16(__builtin_bit_cast(bf16x2, a), __builtin_bit_cast(bf16x2, b), acc, false);
}
__device__ __forceinline__ unsigned perm_lo(unsigned a, unsigned b) { return __builtin_amdgcn_perm(b, a, 0x05040100u); }
__device__ __forceinline__ unsigned perm_hi(unsigned a, unsigned b) { return __builtin_amdgcn_perm(b, a, 0x07060302u); }
template <int CTRL> __device__ __forceinline__ float dpp(float x) {
  return __builtin_bit_cast(float, __builtin_amdgcn_mov_dpp(__builtin_bit_cast(int, x), CTRL, 0xf, 0xf, true));
}
constexpr int XOR1 = 0xB1, XOR2 = 0x4E, XOR7 = 0x141, XOR8 = 0x128, ROR4 = 0x124;
__device__ __forceinline__ float swap16_add(float a, float b) {
  auto s = __builtin_amdgcn_permlane16_swap(__float_as_uint(a), __float_as_uint(b), false, false);
  return __uint_as_float(s[0]) + __uint_as_float(s[1]);
}
__device__ __forceinline__ float swap32_add(float a, float b) {
  auto s = __builtin_amdgcn_permlane32_swap(__float_as_uint(a), __float_as_uint(b), false, false);
  return __uint_as_float(s[0]) + __uint_as_float(s[1]);
}
template <int CTRL> __device__ __forceinline__ float comb(float a, float b, bool sel) {
  const float t1 = a + dpp<CTRL>(a), t2 = b + dpp<CTRL>(b);
  return sel ? t2 : t1;
}
__device__ __forceinline__ float wave_sum(float v) {
  v += dpp<XOR1>(v); v += dpp<XOR2>(v); v += dpp<XOR7>(v); v += dpp<XOR8>(v);
  v = swap16_add(v, v); return swap32_add(v, v);
}
__device__ __forceinline__ float wave_max(float v) {
  v = fmaxf(v, dpp<XOR1>(v)); v = fmaxf(v, dpp<XOR2>(v)); v = fmaxf(v, dpp<XOR7>(v)); v = fmaxf(v, dpp<XOR8>(v));
  auto s = __builtin_amdgcn_permlane16_swap(__float_as_uint(v), __float_as_uint(v), false, false);
  v = fmaxf(__uint_as_float(s[0]), __uint_as_float(s[1]));
  auto t = __builtin_amdgcn_permlane32_swap(__float_as_uint(v), __float_as_uint(v), false, false);
  return fmaxf(__uint_as_float(t[0]), __uint_as_float(t[1]));
}
__device__ __forceinline__ void wg_barrier() { asm volatile("s_waitcnt lgkmcnt(0)\n\ts_barrier" ::: "memory"); }
#define SB() __builtin_amdgcn_sched_barrier(0)
#define PIN(x) asm volatile("" : "+v"(x))

struct Args {
  const unsigned short* x; const unsigned short* K; const unsigned short* V; const unsigned short* wq; const unsigned short* wo;
  const unsigned short* wg; const unsigned short* wu; const unsigned short* wd;
  float* ypart; float* yffn; float* ml;
};

struct __attribute__((aligned(16))) Lds {
  float qpart[8][DH];
  float so[4][DH];
  float sp[4][2][128];
  float sy[8][D];
  float syf[8][D];
  float sred[8][64];
  unsigned xn[224];
  float ml[4][2];
};

__global__ void __launch_bounds__(768) k_main(Args a) {
  __shared__ Lds s;
  const int t = threadIdx.x, lane = t & 63, w = __builtin_amdgcn_readfirstlane(t >> 6);
  const int c = lane & 31, hf = lane >> 5;
  const int blk = blockIdx.x, xc = blk & 7, r = blk >> 3;
  const int h = 2 * xc + (r & 1), b = r >> 3, sidx = (r >> 1) & 3;
  const int id = (b * NH + h) * 4 + sidx;
  const int idx8 = (h & 1) * 4 + sidx, chunk = 7 * xc + idx8;
  const bool has_ffn = idx8 < 7;

  if (w < 4) {
    const size_t base = (size_t)id * (1024 * DH) + (size_t)w * 512;
    const unsigned short* kb = a.K + base;
    const unsigned short* vb = a.V + base;
    const int rot = 0;
    const int vo = lane * 16;
    const unsigned span = 512 * 1024 - w * 1024;
    u32x4 buf[32];
    {
      const rsrc_t rk = mk_rsrc(kb, span);
#pragma unroll
      for (int j = 0; j < 32; ++j) buf[j] = ld_nt(rk, vo, ((rot + j) & 127) * 4096);
    }
    SB();
    wg_barrier();
    unsigned qf[4];
    {
      f32x4 q0 = {0, 0, 0, 0}, q1 = {0, 0, 0, 0};
#pragma unroll
      for (int k = 0; k < 8; ++k) { q0 += *(const f32x4*)&s.qpart[k][8 * c]; q1 += *(const f32x4*)&s.qpart[k][8 * c + 4]; }
      qf[0] = cvtpk(q0[0], q0[1]); qf[1] = cvtpk(q0[2], q0[3]); qf[2] = cvtpk(q1[0], q1[1]); qf[3] = cvtpk(q1[2], q1[3]);
    }
    const bool c8 = c & 8, c4 = c & 4, c2 = c & 2, c1 = c & 1;
    float s0 = 0.f, s1 = 0.f, s2 = 0.f, s3 = 0.f;
#pragma unroll 1
    for (int g = 0; g < 4; ++g) {
      const rsrc_t rn = mk_rsrc(g < 3 ? kb : vb, span);
      const int i0 = rot + 32 * (g + 1);
      float p0, p1, p2, p3, p4, d;
#pragma unroll
      for (int j = 0; j < 32; ++j) {
        d = dot2(buf[j][0], qf[0], 0.f); d = dot2(buf[j][1], qf[1], d); d = dot2(buf[j][2], qf[2], d); d = dot2(buf[j][3], qf[3], d);
        PIN(d); SB();
        buf[j] = ld_nt(rn, vo, ((i0 + j) & 127) * 4096);
        if (!(j & 1)) p0 = d; else { d = swap16_add(p0, d);
          if (!(j & 2)) p1 = d; else { d = comb<XOR8>(p1, d, c8);
            if (!(j & 4)) p2 = d; else { d = comb<XOR7>(p2, d, c4);
              if (!(j & 8)) p3 = d; else { d = comb<XOR2>(p3, d, c2);
                if (!(j & 16)) p4 = d; else d = comb<XOR1>(p4, d, c1); } } } }
        SB();
      }
      s3 = s2; s2 = s1; s1 = s0; s0 = rbf(d) * 0.0625f;
    }
    const float m = wave_max(fmaxf(fmaxf(s0, s1), fmaxf(s2, s3)));
    const float e0 = __expf(s3 - m), e1 = __expf(s2 - m), e2 = __expf(s1 - m), e3 = __expf(s0 - m);
    const int br = __builtin_bitreverse32((unsigned)c) >> 27;
    float* sp = &s.sp[w][hf][0];
    sp[br] = e0; sp[32 + br] = e1; sp[64 + br] = e2; sp[96 + br] = e3;
    const float l = wave_sum((e0 + e1) + (e2 + e3));
    if (lane == 0) { s.ml[w][0] = m; s.ml[w][1] = l; }
    wg_barrier();
    f32x2 acc[4] = {{0, 0}, {0, 0}, {0, 0}, {0, 0}};
    const f32x4* sp4 = (const f32x4*)sp;
    f32x4 pc, pn = sp4[0];
#define VSTEP(j_) { if (!((j_) & 3)) { pc = pn; pn = sp4[(g * 8 + (j_) / 4 + 1) & 31]; } \
        const u32x4 v = buf[j_]; const float p = pc[(j_) & 3]; const f32x2 pp = {p, p}; \
        _Pragma("unroll") for (int k = 0; k < 4; ++k) { const f32x2 vv = {lo_f(v[k]), hi_f(v[k])}; acc[k] = __builtin_elementwise_fma(vv, pp, acc[k]); PIN(acc[k]); } }
#pragma unroll 1
    for (int g = 0; g < 3; ++g) {
      const rsrc_t rv = mk_rsrc(vb, span);
      const int i0 = rot + 32 * (g + 1);
#pragma unroll
      for (int j = 0; j < 32; ++j) { VSTEP(j); SB(); buf[j] = ld_nt(rv, vo, ((i0 + j) & 127) * 4096); SB(); }
    }
    {
      const int g = 3;
#pragma unroll
      for (int j = 0; j < 32; ++j) { VSTEP(j); SB(); }
    }
#undef VSTEP
    {
      f32x4 o;
      o[0] = swap32_add(acc[0][0], acc[2][0]); o[1] = swap32_add(acc[0][1], acc[2][1]);
      o[2] = swap32_add(acc[1][0], acc[3][0]); o[3] = swap32_add(acc[1][1], acc[3][1]);
      *(f32x4*)&s.so[w][8 * c + 4 * hf] = o;
    }
    wg_barrier();
    if (w >= 2) {
      const int u = t - 128;
      if (has_ffn && u < 112) {
        f32x4 y = {0, 0, 0, 0};
#pragma unroll
        for (int k = 0; k < 8; ++k) y += *(const f32x4*)&s.syf[k][4 * u];
        *(f32x4*)&a.yffn[(size_t)(b * NCHUNK + chunk) * D + 4 * u] = y;
      }
      if (u == 127) {
        float M = fmaxf(fmaxf(s.ml[0][0], s.ml[1][0]), fmaxf(s.ml[2][0], s.ml[3][0])), L = 0.f;
#pragma unroll
        for (int k = 0; k < 4; ++k) L = fmaf(__expf(s.ml[k][0] - M), s.ml[k][1], L);
        *(f32x2*)&a.ml[id * 2] = f32x2{M, L};
      }
    }
    wg_barrier();
    if (t < 112) {
      f32x4 y = {0, 0, 0, 0};
#pragma unroll
      for (int k = 0; k < 8; ++k) y += *(const f32x4*)&s.sy[k][4 * t];
      *(f32x4*)&a.ypart[(size_t)id * D + 4 * t] = y;
    }
  } else {
    const int k = w - 4;
    const int vrow = lane < 56 ? lane * 16 : 0x7ffffff0 - 0x1000000;
    const rsrc_t rx = mk_rsrc(a.x + b * D, D * 2);
    const u32x4 xr = ld_c(rx, lane * 16, 0);
    const rsrc_t rq = mk_rsrc(a.wq, D * HD * 2);
    u32x4 wv[32];
    {
      const int vo = (56 * k + 28 * hf) * (HD * 2) + h * (DH * 2) + c * 16;
#pragma unroll
      for (int i = 0; i < 28; ++i) wv[i] = ld_c(rq, vo, i * (HD * 2));
    }
    SB();
    {
      float v[8];
#pragma unroll
      for (int e = 0; e < 4; ++e) { v[2 * e] = lo_f(xr[e]); v[2 * e + 1] = hi_f(xr[e]); }
      const float mean = wave_sum(((v[0] + v[1]) + (v[2] + v[3])) + ((v[4] + v[5]) + (v[6] + v[7]))) * (1.f / D);
      float q = 0.f;
#pragma unroll
      for (int e = 0; e < 8; ++e) { const float dd = v[e] - mean; q = fmaf(dd, dd, q); }
      q = wave_sum(lane < 56 ? q : 0.f);
      const float var = rbf(q * (1.f / D));
      const float den = rbf(sqrtf(rbf(var + 1e-5f)));
      const float mb = rbf(mean);
      u32x4 xn;
#pragma unroll
      for (int e = 0; e < 4; ++e) xn[e] = cvtpk(rbf(v[2 * e] - mb) / den, rbf(v[2 * e + 1] - mb) / den);
      if (lane < 56) *(u32x4*)&s.xn[4 * lane] = xn;
      asm volatile("s_waitcnt lgkmcnt(0)" ::: "memory");
    }
    {
      float acc[8] = {0, 0, 0, 0, 0, 0, 0, 0};
      const u32x2* xp = (const u32x2*)&s.xn[28 * k + 14 * hf];
#pragma unroll
      for (int mm = 0; mm < 7; ++mm) {
        const u32x2 x2 = xp[mm];
#pragma unroll
        for (int u = 0; u < 2; ++u) {
          const u32x4 A = wv[4 * mm + 2 * u], B = wv[4 * mm + 2 * u + 1];
#pragma unroll
          for (int e = 0; e < 4; ++e) {
            acc[2 * e] = dot2(perm_lo(A[e], B[e]), x2[u], acc[2 * e]);
            acc[2 * e + 1] = dot2(perm_hi(A[e], B[e]), x2[u], acc[2 * e + 1]);
          }
        }
      }
      f32x4 o;
#pragma unroll
      for (int e = 0; e < 4; ++e) o[e] = swap32_add(acc[e], acc[4 + e]);
      *(f32x4*)&s.qpart[k][8 * c + 4 * hf] = o;
    }
    wg_barrier();
    if (has_ffn && k < 7) {
      const rsrc_t rg = mk_rsrc(a.wg, D * F * 2), ru = mk_rsrc(a.wu, D * F * 2);
      const int row0 = 64 * k + (lane >> 2);
      const int vo = row0 * (F * 2) + chunk * 64 + (lane & 3) * 16;
      u32x4 gw[4], uw[4];
#pragma unroll
      for (int i = 0; i < 4; ++i) { gw[i] = ld_c(rg, vo, i * 16 * (F * 2)); uw[i] = ld_c(ru, vo, i * 16 * (F * 2)); }
      float ag[8] = {0, 0, 0, 0, 0, 0, 0, 0}, au[8] = {0, 0, 0, 0, 0, 0, 0, 0};
#pragma unroll
      for (int i = 0; i < 4; ++i) {
        const float xv = __uint_as_float((unsigned)((const unsigned short*)s.xn)[row0 + 16 * i] << 16);
#pragma unroll
        for (int e = 0; e < 4; ++e) {
          ag[2 * e] = fmaf(xv, lo_f(gw[i][e]), ag[2 * e]); ag[2 * e + 1] = fmaf(xv, hi_f(gw[i][e]), ag[2 * e + 1]);
          au[2 * e] = fmaf(xv, lo_f(uw[i][e]), au[2 * e]); au[2 * e + 1] = fmaf(xv, hi_f(uw[i][e]), au[2 * e + 1]);
        }
      }
      float v8[8];
#pragma unroll
      for (int e = 0; e < 8; ++e) v8[e] = swap32_add(ag[e], au[e]);
      f32x4 v4;
#pragma unroll
      for (int e = 0; e < 4; ++e) { float z = swap16_add(v8[e], v8[4 + e]); z += dpp<XOR8>(z); z += dpp<ROR4>(z); v4[e] = z; }
      if ((lane & 12) == 0) *(f32x4*)&s.sred[k][hf * 32 + 8 * (lane & 3) + 4 * ((lane >> 4) & 1)] = v4;
    }
    wg_barrier();
    if (has_ffn) {
      const rsrc_t rd = mk_rsrc(a.wd + (size_t)(chunk * 32 + 4 * k) * D, 4 * D * 2);
      u32x4 dw[4];
#pragma unroll
      for (int i = 0; i < 4; ++i) dw[i] = ld_c(rd, vrow, i * (D * 2));
      const int col = 4 * k + (lane & 3);
      float g = 0.f, u = 0.f;
#pragma unroll
      for (int kk = 0; kk < 7; ++kk) { g += s.sred[kk][col]; u += s.sred[kk][32 + col]; }
      g = rbf(g); u = rbf(u);
      const float inner = 0.7978845608028654f * (g + 0.044715f * g * g * g);
      const float ge = rbf(0.5f * g * (1.f + tanhf(inner)));
      const float hv = rbf(ge * u);
      const unsigned h01 = cvtpk(dpp<0x00>(hv), dpp<0x55>(hv)), h23 = cvtpk(dpp<0xAA>(hv), dpp<0xFF>(hv));
      f32x4 y0, y1;
#pragma unroll
      for (int e = 0; e < 2; ++e) {
        y0[2 * e] = dot2(perm_lo(dw[2][e], dw[3][e]), h23, dot2(perm_lo(dw[0][e], dw[1][e]), h01, 0.f));
        y0[2 * e + 1] = dot2(perm_hi(dw[2][e], dw[3][e]), h23, dot2(perm_hi(dw[0][e], dw[1][e]), h01, 0.f));
        y1[2 * e] = dot2(perm_lo(dw[2][e + 2], dw[3][e + 2]), h23, dot2(perm_lo(dw[0][e + 2], dw[1][e + 2]), h01, 0.f));
        y1[2 * e + 1] = dot2(perm_hi(dw[2][e + 2], dw[3][e + 2]), h23, dot2(perm_hi(dw[0][e + 2], dw[1][e + 2]), h01, 0.f));
      }
      if (lane < 56) { *(f32x4*)&s.syf[k][8 * lane] = y0; *(f32x4*)&s.syf[k][8 * lane + 4] = y1; }
    }
    {
      const rsrc_t ro = mk_rsrc(a.wo + (size_t)(h * DH + 32 * k) * D, 32 * D * 2);
#pragma unroll
      for (int i = 0; i < 32; ++i) wv[i] = ld_c(ro, vrow, i * (D * 2));
#pragma unroll
      for (int p = 0; p < 16; ++p)
#pragma unroll
        for (int e = 0; e < 4; ++e) {
          unsigned lo = perm_lo(wv[2 * p][e], wv[2 * p + 1][e]), hi = perm_hi(wv[2 * p][e], wv[2 * p + 1][e]);
          PIN(lo); PIN(hi);
          wv[2 * p][e] = lo; wv[2 * p + 1][e] = hi;
        }
    }
    wg_barrier();
    {
      const float m0 = s.ml[0][0], m1 = s.ml[1][0], m2 = s.ml[2][0], m3 = s.ml[3][0];
      const float M = fmaxf(fmaxf(m0, m1), fmaxf(m2, m3));
      const int d0 = 32 * k + 2 * (lane & 15);
      const f32x2 a0 = *(const f32x2*)&s.so[0][d0], a1 = *(const f32x2*)&s.so[1][d0], a2 = *(const f32x2*)&s.so[2][d0], a3 = *(const f32x2*)&s.so[3][d0];
      const float e0 = __expf(m0 - M), e1 = __expf(m1 - M), e2 = __expf(m2 - M), e3 = __expf(m3 - M);
      const f32x2 o = a0 * e0 + a1 * e1 + a2 * e2 + a3 * e3;
      const unsigned opk = cvtpk(o[0], o[1]);
      float acc[8] = {0, 0, 0, 0, 0, 0, 0, 0};
#pragma unroll
      for (int p = 0; p < 16; ++p) {
        const unsigned op = __builtin_amdgcn_readlane(opk, p);
#pragma unroll
        for (int e = 0; e < 4; ++e) { acc[2 * e] = dot2(wv[2 * p][e], op, acc[2 * e]); acc[2 * e + 1] = dot2(wv[2 * p + 1][e], op, acc[2 * e + 1]); }
      }
      if (lane < 56) { *(f32x4*)&s.sy[k][8 * lane] = f32x4{acc[0], acc[1], acc[2], acc[3]}; *(f32x4*)&s.sy[k][8 * lane + 4] = f32x4{acc[4], acc[5], acc[6], acc[7]}; }
    }
    wg_barrier();
  }
}

__global__ void __launch_bounds__(256) k_final(const unsigned short* x, const float* ypart, const float* yffn, const float* ml, unsigned* out) {
  const int lane = threadIdx.x & 63, wv = blockIdx.x * 4 + (threadIdx.x >> 6);
  const int b = wv / 112, cq = wv - b * 112;
  const f32x4 y = *(const f32x4*)&ypart[(size_t)(b * 64 + lane) * D + 4 * cq];
  const f32x2 mlv = *(const f32x2*)&ml[(b * 64 + lane) * 2];
  f32x4 f = {0, 0, 0, 0};
  if (lane < NCHUNK) f = *(const f32x4*)&yffn[(size_t)(b * NCHUNK + lane) * D + 4 * cq];
  const unsigned xw = *(const unsigned*)&x[b * D + 4 * cq + 2 * ((lane >> 4) & 1)];
  float M = fmaxf(mlv[0], dpp<XOR1>(mlv[0])); M = fmaxf(M, dpp<XOR2>(M));
  const float e = __expf(mlv[0] - M);
  float L = e * mlv[1]; L += dpp<XOR1>(L); L += dpp<XOR2>(L);
  const float wt = e / L;
  float v[4];
#pragma unroll
  for (int i = 0; i < 4; ++i) v[i] = swap32_add(y[i] * wt, f[i]);
  float z0 = swap16_add(v[0], v[2]), z1 = swap16_add(v[1], v[3]);
  z0 += dpp<XOR1>(z0); z0 += dpp<XOR2>(z0); z0 += dpp<XOR7>(z0); z0 += dpp<XOR8>(z0);
  z1 += dpp<XOR1>(z1); z1 += dpp<XOR2>(z1); z1 += dpp<XOR7>(z1); z1 += dpp<XOR8>(z1);
  auto s0 = __builtin_amdgcn_permlane32_swap(__float_as_uint(z0), __float_as_uint(z0), false, false);
  auto s1 = __builtin_amdgcn_permlane32_swap(__float_as_uint(z1), __float_as_uint(z1), false, false);
  const float r0 = rbf(lo_f(xw) + rbf(__uint_as_float(s0[0]))) + rbf(__uint_as_float(s0[1]));
  const float r1 = rbf(hi_f(xw) + rbf(__uint_as_float(s1[0]))) + rbf(__uint_as_float(s1[1]));
  if ((lane & 47) == 0) out[(b * D + 4 * cq) / 2 + (lane >> 4)] = cvtpk(r0, r1);
}
}

extern "C" void kernel_launch(void* const* d_in, const int* in_sizes, int n_in, void* d_out, int out_size, void* d_ws,
                              size_t ws_size, hipStream_t stream) {
  char* ws = (char*)d_ws;
  Args a;
  a.x = (const unsigned short*)d_in[0]; a.K = (const unsigned short*)d_in[1]; a.V = (const unsigned short*)d_in[2];
  a.wq = (const unsigned short*)d_in[3]; a.wo = (const unsigned short*)d_in[4];
  a.wg = (const unsigned short*)d_in[6]; a.wu = (const unsigned short*)d_in[7]; a.wd = (const unsigned short*)d_in[8];
  a.ypart = (float*)ws;
  a.yffn = (float*)(ws + 512 * 1024);
  a.ml = (float*)(ws + 1024 * 1024);
  k_main<<<256, 768, 0, stream>>>(a);
  k_final<<<112, 256, 0, stream>>>(a.x, a.ypart, a.yffn, a.ml, (unsigned*)d_out);
}
```

```cpp
#include <hip/hip_runtime.h>
#include <hip/hip_bf16.h>
#include <stdint.h>

#define W0 32
#define WV 16
#define TH 32
#define CDIV 2
#define HPRIO 3
#define SPIN_MAX 100000
#define ROTF ((blk >> 1) & 127)
#define X_FFN 1
#define WOB 4

namespace {
constexpr int D = 448, NH = 16, DH = 256, F = 1792, HD = NH * DH;
constexpr unsigned WS_E = 0;
constexpr unsigned WS_A = 4096;
constexpr unsigned WS_M = WS_A + 524288;
constexpr unsigned WS_F = WS_M + 4096;
constexpr unsigned WS_D = WS_F + 28672;
constexpr unsigned WS_B = WS_D + 114688;
constexpr unsigned WS_C = WS_B + 524288;
constexpr unsigned WS_END = WS_C + 229376;
constexpr int OOB = 0x40000000;

typedef unsigned u32x4 __attribute__((ext_vector_type(4)));
typedef unsigned u32x2 __attribute__((ext_vector_type(2)));
typedef float f32x4 __attribute__((ext_vector_type(4)));
typedef float f32x2 __attribute__((ext_vector_type(2)));
typedef __bf16 bf16x2 __attribute__((ext_vector_type(2)));
typedef __amdgpu_buffer_rsrc_t rsrc_t;

__device__ __forceinline__ rsrc_t mk_rsrc(const void* p, unsigned bytes) {
  return __builtin_amdgcn_make_buffer_rsrc((void*)p, 0, bytes, 0x00020000);
}
__device__ __forceinline__ u32x4 ld_nt(rsrc_t r, int voff, int soff) { return __builtin_amdgcn_raw_buffer_load_b128(r, voff, soff, 2); }
__device__ __forceinline__ u32x4 ld_c(rsrc_t r, int voff, int soff) { return __builtin_amdgcn_raw_buffer_load_b128(r, voff, soff, 0); }
__device__ __forceinline__ u32x4 ld_sc(rsrc_t r, int voff, int soff) { return __builtin_amdgcn_raw_buffer_load_b128(r, voff, soff, 16); }
__device__ __forceinline__ void st_sc(rsrc_t r, int voff, int soff, float a, float b, unsigned tag) {
  __builtin_amdgcn_raw_buffer_store_b128(u32x4{__float_as_uint(a), tag, __float_as_uint(b), tag}, r, voff, soff, 16);
}
__device__ __forceinline__ float lo_f(unsigned w) { return __uint_as_float(w << 16); }
__device__ __forceinline__ float hi_f(unsigned w) { return __uint_as_float(w & 0xffff0000u); }
__device__ __forceinline__ unsigned cvtpk(float lo, float hi) {
  const bf16x2 r = {(__bf16)lo, (__bf16)hi}; return __builtin_bit_cast(unsigned, r);
}
__device__ __forceinline__ float rbf(float x) { return lo_f(cvtpk(x, x)); }
__device__ __forceinline__ float dot2(unsigned a, unsigned b, float acc) {
  return __builtin_amdgcn_fdot2_f32_bf16(__builtin_bit_cast(bf16x2, a), __builtin_bit_cast(bf16x2, b), acc, false);
}
__device__ __forceinline__ unsigned perm_lo(unsigned a, unsigned b) { return __builtin_amdgcn_perm(b, a, 0x05040100u); }
__device__ __forceinline__ unsigned perm_hi(unsigned a, unsigned b) { return __builtin_amdgcn_perm(b, a, 0x07060302u); }
template <int CTRL> __device__ __forceinline__ float dpp(float x) {
  return __builtin_bit_cast(float, __builtin_amdgcn_mov_dpp(__builtin_bit_cast(int, x), CTRL, 0xf, 0xf, true));
}
constexpr int XOR1 = 0xB1, XOR2 = 0x4E, XOR7 = 0x141, XOR8 = 0x128;
__device__ __forceinline__ float swap16_add(float a, float b) {
  auto s = __builtin_amdgcn_permlane16_swap(__float_as_uint(a), __float_as_uint(b), false, false);
  return __uint_as_float(s[0]) + __uint_as_float(s[1]);
}
__device__ __forceinline__ float swap32_add(float a, float b) {
  auto s = __builtin_amdgcn_permlane32_swap(__float_as_uint(a), __float_as_uint(b), false, false);
  return __uint_as_float(s[0]) + __uint_as_float(s[1]);
}
__device__ __forceinline__ float swap16_max(float v) {
  auto s = __builtin_amdgcn_permlane16_swap(__float_as_uint(v), __float_as_uint(v), false, false);
  return fmaxf(__uint_as_float(s[0]), __uint_as_float(s[1]));
}
__device__ __forceinline__ float swap32_max(float v) {
  auto s = __builtin_amdgcn_permlane32_swap(__float_as_uint(v), __float_as_uint(v), false, false);
  return fmaxf(__uint_as_float(s[0]), __uint_as_float(s[1]));
}
template <int CTRL> __device__ __forceinline__ float comb(float a, float b, bool sel) {
  const float t1 = a + dpp<CTRL>(a), t2 = b + dpp<CTRL>(b);
  return sel ? t2 : t1;
}
__device__ __forceinline__ float row_sum(float v) { v += dpp<XOR1>(v); v += dpp<XOR2>(v); v += dpp<XOR7>(v); return v + dpp<XOR8>(v); }
__device__ __forceinline__ float hi3_sum(float v) { v += dpp<XOR8>(v); v = swap16_add(v, v); return swap32_add(v, v); }
__device__ __forceinline__ float hi3_max(float v) { v = fmaxf(v, dpp<XOR8>(v)); return swap32_max(swap16_max(v)); }
__device__ __forceinline__ float wave_sum(float v) { v = row_sum(v); v = swap16_add(v, v); return swap32_add(v, v); }
__device__ __forceinline__ float wave_max(float v) {
  v = fmaxf(v, dpp<XOR1>(v)); v = fmaxf(v, dpp<XOR2>(v)); v = fmaxf(v, dpp<XOR7>(v)); return hi3_max(v);
}
__device__ __forceinline__ void wg_barrier() { asm volatile("s_waitcnt lgkmcnt(0)\n\ts_barrier" ::: "memory"); }
#define SB() __builtin_amdgcn_sched_barrier(0)
#define PIN(x) asm volatile("" : "+v"(x))
#define FENCE() asm volatile("" ::: "memory")
#define TS(slot) do {} while (0)

__device__ __forceinline__ void red32(const float (&acc)[4][8], bool bit3, float (&v)[4]) {
  float u[8];
#pragma unroll
  for (int e = 0; e < 8; ++e) u[e] = swap16_add(swap32_add(acc[0][e], acc[2][e]), swap32_add(acc[1][e], acc[3][e]));
  v[0] = comb<XOR8>(u[0], u[2], bit3); v[1] = comb<XOR8>(u[1], u[3], bit3);
  v[2] = comb<XOR8>(u[4], u[6], bit3); v[3] = comb<XOR8>(u[5], u[7], bit3);
}
__device__ __forceinline__ f32x2 red32w(const float (&acc)[4][8], bool bit3, bool bit2) {
  float u[8], t[4];
#pragma unroll
  for (int e = 0; e < 8; ++e) u[e] = swap16_add(swap32_add(acc[0][e], acc[2][e]), swap32_add(acc[1][e], acc[3][e]));
#pragma unroll
  for (int e = 0; e < 4; ++e) t[e] = comb<XOR8>(u[e], u[e + 4], bit3);
  f32x2 r;
#pragma unroll
  for (int e = 0; e < 2; ++e) {
    int x = __builtin_amdgcn_update_dpp(0, __float_as_int(t[e]), 0x104, 0xf, 0x5, false);
    x = __builtin_amdgcn_update_dpp(x, __float_as_int(t[e + 2]), 0x114, 0xf, 0xa, false);
    r[e] = (bit2 ? t[e + 2] : t[e]) + __int_as_float(x);
  }
  return r;
}
__device__ __forceinline__ void mac32(float (&acc)[4][8], u32x4 A, u32x4 B, u32x4 x4) {
#pragma unroll
  for (int e = 0; e < 4; ++e) {
    const unsigned pl = perm_lo(A[e], B[e]), ph = perm_hi(A[e], B[e]);
#pragma unroll
    for (int bb = 0; bb < 4; ++bb) { acc[bb][2 * e] = dot2(pl, x4[bb], acc[bb][2 * e]); acc[bb][2 * e + 1] = dot2(ph, x4[bb], acc[bb][2 * e + 1]); }
  }
}
template <int N, int SLEEP> __device__ __forceinline__ void poll(u32x4 (&v)[N], rsrc_t r, int voff, int soff, int stride, unsigned tag, bool skip_last = false) {
#pragma unroll 1
  for (int it = 0; it < SPIN_MAX; ++it) {
    FENCE();
#pragma unroll
    for (int i = 0; i < N; ++i) v[i] = ld_sc(r, voff, soff + i * stride);
    bool ok = true;
#pragma unroll
    for (int i = 0; i < N; ++i) { const bool g = v[i][1] == tag && v[i][3] == tag; ok = ok && (g || (i == N - 1 && skip_last)); }
    if (__builtin_amdgcn_ballot_w64(!ok) == 0) break;
    if (SLEEP) __builtin_amdgcn_s_sleep(SLEEP);
  }
}
template <int SLEEP> __device__ __forceinline__ void lds_wait(int* p, int n) {
#pragma unroll 1
  for (int it = 0; it < SPIN_MAX * 64; ++it) {
    FENCE();
    if (__builtin_amdgcn_readfirstlane(__hip_atomic_load(p, __ATOMIC_RELAXED, __HIP_MEMORY_SCOPE_WORKGROUP)) >= n) break;
    if (SLEEP) __builtin_amdgcn_s_sleep(SLEEP);
  }
  FENCE();
}
__device__ __forceinline__ void flag_sync(u32x4* f, int w, int lane, unsigned tag) {
  FENCE();
  if (lane == 0) __hip_atomic_store((unsigned*)f + w, tag, __ATOMIC_RELAXED, __HIP_MEMORY_SCOPE_WORKGROUP);
#pragma unroll 1
  for (int it = 0; it < SPIN_MAX * 64; ++it) {
    FENCE();
    const unsigned v = __hip_atomic_load((unsigned*)f + (lane & 3), __ATOMIC_RELAXED, __HIP_MEMORY_SCOPE_WORKGROUP);
    if (__builtin_amdgcn_ballot_w64(v != tag) == 0) break;
  }
  FENCE();
}
__device__ __forceinline__ int lds_arrive(int* p, int lane) {
  FENCE();
  int n = 0;
  if (lane == 0) n = __hip_atomic_fetch_add(p, 1, __ATOMIC_RELAXED, __HIP_MEMORY_SCOPE_WORKGROUP);
  n = __builtin_amdgcn_readfirstlane(n);
  FENCE();
  return n;
}
__device__ __forceinline__ void lds_signal(int* p, int lane) {
  FENCE();
  if (lane == 0) __hip_atomic_fetch_add(p, 1, __ATOMIC_RELAXED, __HIP_MEMORY_SCOPE_WORKGROUP);
  FENCE();
}

struct Args {
  const unsigned short* x; const unsigned short* K; const unsigned short* V; const unsigned short* wq; const unsigned short* wo;
  const unsigned short* wg; const unsigned short* wu; const unsigned short* wd;
  unsigned* out; char* ws;
};

struct __attribute__((aligned(16))) Lds {
  float sp[4][2][128];
  float so[4][DH];
  float sy[4][112];
  float sf[4];
  unsigned xn[224][4];
  unsigned hT[112][4];
  float fp[4][256];
  f32x4 qp[4][64];
  unsigned q[128];
  u32x4 fl[2];
  float ml[4][2];
  int cntK, cntV, cntH, cntF; unsigned tag;
};

template <int V> struct IC { static constexpr int value = V; };
template <int N, int I = 0, class Fn> __device__ __forceinline__ void static_for(Fn&& f) {
  if constexpr (I < N) { f(IC<I>{}); static_for<N, I + 1>(f); }
}
constexpr int issued(int j) {
  return j < 0 ? W0 : (W0 + (j + 1) / CDIV > j + 1 + WV ? W0 + (j + 1) / CDIV : j + 1 + WV);
}

__global__ void __launch_bounds__(512, 2) k_main(Args a) {
  __shared__ Lds s;
  const int t = threadIdx.x, lane = t & 63, w = __builtin_amdgcn_readfirstlane(t >> 6);
  const int blk = blockIdx.x, xc = blk & 7, r = blk >> 3;
  const int h = 2 * xc + (r & 1), b = r >> 3, sidx = (r >> 1) & 3;
  const int bh = b * NH + h, id = bh * 4 + sidx;
  const rsrc_t rw = mk_rsrc(a.ws, WS_END);

  if (w >= 4) {
    const int ws_ = w - 4;
    const int c = lane & 31, hf = lane >> 5;
    const size_t base = (size_t)id * (1024 * DH) + (size_t)ws_ * 512;
    const unsigned span = 512 * 1024 - ws_ * 1024;
    const rsrc_t rk = mk_rsrc(a.K + base, span), rv = mk_rsrc(a.V + base, span);
    const int rot = ROTF;
    int off = rot * 4096;
    const int vo = lane * 16;
    if (ws_ == 0) TS(0);
    u32x4 kb[128], vb[WV];
    static_for<W0>([&](auto J) __attribute__((always_inline)) {
      constexpr int j = J.value;
      if constexpr (j >= TH) { PIN(kb[j - TH]); SB(); }
      kb[j] = ld_nt(rk, vo, off); off = (off + 4096) & 0x7ffff;
      SB();
    });
    if (ws_ == 0) TS(1);
    wg_barrier();
    if (ws_ == 0) TS(2);
    const u32x4 qf = *(const u32x4*)&s.q[4 * c];
    const unsigned tag = s.tag;
    const bool c8 = c & 8, c4 = c & 4, c2 = c & 2, c1 = c & 1;
    float sc[4];
    {
      float p0, p1, p2, p3, p4;
      static_for<128>([&](auto J) __attribute__((always_inline)) {
        constexpr int j = J.value;
        float d = dot2(kb[j][0], qf[0], 0.f); d = dot2(kb[j][1], qf[1], d); d = dot2(kb[j][2], qf[2], d); d = dot2(kb[j][3], qf[3], d);
        PIN(d); SB();
        if constexpr (issued(j) > issued(j - 1)) {
          constexpr int n = issued(j - 1);
          if constexpr (n < 128) kb[n] = ld_nt(rk, vo, off); else vb[n - 128] = ld_nt(rv, vo, off);
          off = (off + 4096) & 0x7ffff;
        }
        if constexpr (!(j & 1)) p0 = d; else { d = swap16_add(p0, d);
          if constexpr (!(j & 2)) p1 = d; else { d = comb<XOR8>(p1, d, c8);
            if constexpr (!(j & 4)) p2 = d; else { d = comb<XOR7>(p2, d, c4);
              if constexpr (!(j & 8)) p3 = d; else { d = comb<XOR2>(p3, d, c2);
                if constexpr (!(j & 16)) p4 = d; else { d = comb<XOR1>(p4, d, c1); sc[j >> 5] = rbf(d) * 0.0625f; } } } } }
        SB();
      });
    }
    if (ws_ == 0) TS(3);
    const float m = wave_max(fmaxf(fmaxf(sc[0], sc[1]), fmaxf(sc[2], sc[3])));
    const float e0 = __expf(sc[0] - m), e1 = __expf(sc[1] - m), e2 = __expf(sc[2] - m), e3 = __expf(sc[3] - m);
    const int br = __builtin_bitreverse32((unsigned)c) >> 27;
    float* sp = &s.sp[ws_][hf][0];
    sp[br] = e0; sp[32 + br] = e1; sp[64 + br] = e2; sp[96 + br] = e3;
    const float l = wave_sum((e0 + e1) + (e2 + e3));
    if (lane == 0) { s.ml[ws_][0] = m; s.ml[ws_][1] = l; }
    lds_signal(&s.cntK, lane);
    asm volatile("s_waitcnt lgkmcnt(0)" ::: "memory");
    f32x2 acc[4] = {{0, 0}, {0, 0}, {0, 0}, {0, 0}};
    const f32x4* sp4 = (const f32x4*)sp;
    f32x4 pc, pn = sp4[0];
#define VSTEP(j_) { if (!((j_) & 3)) { pc = pn; pn = sp4[(g * (WV / 4) + (j_) / 4 + 1) & 31]; } \
        const u32x4 v = vb[j_]; const float p = pc[(j_) & 3]; const f32x2 pp = {p, p}; \
        _Pragma("unroll") for (int k = 0; k < 4; ++k) { const f32x2 vv = {lo_f(v[k]), hi_f(v[k])}; acc[k] = __builtin_elementwise_fma(vv, pp, acc[k]); PIN(acc[k]); } }
#pragma unroll 1
    for (int g = 0; g < 128 / WV - 1; ++g) {
#pragma unroll
      for (int j = 0; j < WV; ++j) { VSTEP(j); SB(); vb[j] = ld_nt(rv, vo, off); off = (off + 4096) & 0x7ffff; SB(); }
      if (g == 64 / WV - 1) lds_signal(&s.cntK, lane);
    }
    {
      const int g = 128 / WV - 1;
#pragma unroll
      for (int j = 0; j < WV; ++j) { VSTEP(j); SB(); }
    }
#undef VSTEP
    {
      const float o0 = swap32_add(acc[0][0], acc[1][0]), o1 = swap32_add(acc[0][1], acc[1][1]);
      const float o2 = swap32_add(acc[2][0], acc[3][0]), o3 = swap32_add(acc[2][1], acc[3][1]);
      float* so = &s.so[ws_][8 * c + 2 * hf];
      *(f32x2*)so = f32x2{o0, o1}; *(f32x2*)(so + 4) = f32x2{o2, o3};
    }
    lds_signal(&s.cntV, lane);
    if (ws_ == 0) TS(5);
  } else {
    if (HPRIO) __builtin_amdgcn_s_setprio(HPRIO);
    const int r8 = lane >> 3, c8 = lane & 7;
    const bool bit3 = lane & 8;
    unsigned tag;
    {
      const unsigned epoch = __builtin_amdgcn_raw_buffer_load_b32(rw, 0, WS_E, 16);
      const u32x4 xr = ld_c(mk_rsrc(a.x + w * D, D * 2), lane * 16, 0);
      const rsrc_t rq = mk_rsrc(a.wq, D * HD * 2);
      u32x4 wa[2], wb[2];
      {
        const int vq = 2 * r8 * (HD * 2) + c8 * 16, vq1 = w < 3 ? vq : OOB, sq = (112 * b + 32 * w) * (HD * 2) + (DH * h + 64 * sidx) * 2;
        wa[0] = ld_nt(rq, vq, sq); wb[0] = ld_nt(rq, vq, sq + HD * 2);
        wa[1] = ld_nt(rq, vq1, sq + 16 * (HD * 2)); wb[1] = ld_nt(rq, vq1, sq + 17 * (HD * 2));
      }
      SB();
      {
        f32x2 v[4];
#pragma unroll
        for (int e = 0; e < 4; ++e) v[e] = f32x2{lo_f(xr[e]), hi_f(xr[e])};
        const f32x2 s2 = (v[0] + v[1]) + (v[2] + v[3]);
        const float mean = wave_sum(s2[0] + s2[1]) * (1.f / D);
        f32x2 q2 = {0, 0};
#pragma unroll
        for (int e = 0; e < 4; ++e) { const f32x2 dd = v[e] - mean; q2 = __builtin_elementwise_fma(dd, dd, q2); }
        const float var = rbf(wave_sum(lane < 56 ? q2[0] + q2[1] : 0.f) * (1.f / D));
        const float den = rbf(sqrtf(rbf(var + 1e-5f)));
        const float rinv = __builtin_amdgcn_rcpf(den);
        const float mb = rbf(mean);
        if (lane < 56) {
#pragma unroll
          for (int e = 0; e < 4; ++e) {
            const f32x2 dd = v[e] - mb;
            const unsigned tt = cvtpk(dd[0], dd[1]);
            s.xn[4 * lane + e][w] = cvtpk(lo_f(tt) * rinv, hi_f(tt) * rinv);
          }
        }
      }
      tag = __builtin_amdgcn_readfirstlane(epoch) * 0x9E3779B1u + 0x7F4A7C15u;
      flag_sync(&s.fl[0], w, lane, tag);
      if (w == 0) TS(10);
      {
        float acc[4][8] = {};
        mac32(acc, wa[0], wb[0], *(const u32x4*)&s.xn[56 * b + 16 * w + r8][0]);
        mac32(acc, wa[1], wb[1], *(const u32x4*)&s.xn[56 * b + (w < 3 ? 16 * w + 8 : 48) + r8][0]);
        float v[4];
        red32(acc, bit3, v);
        s.qp[w][lane] = f32x4{v[0], v[1], v[2], v[3]};
      }
      flag_sync(&s.fl[1], w, lane, tag);
      {
        const int L = 16 * w + (lane & 15), k = (lane >> 4) & 1;
        const f32x2* p = (const f32x2*)&s.qp[0][L] + k;
        const f32x2 y = (p[0] + p[128]) + (p[256] + p[384]);
        st_sc(rw, lane < 32 ? c8 * 64 + k * 32 + (bit3 ? 16 : 0) : OOB, WS_A + (h * 4 + w) * 8192 + (sidx * 4 + b) * 512, y[0], y[1], tag);
      }
      if (w == 0) TS(11);
      {
        u32x4 v[2];
        poll<2, 0>(v, rw, lane * 16, WS_A + (h * 4 + b) * 8192 + w * 2048, 1024, tag);
        const float q0 = __uint_as_float(v[0][0]) + __uint_as_float(v[1][0]), q1 = __uint_as_float(v[0][2]) + __uint_as_float(v[1][2]);
        const unsigned qq = cvtpk(swap32_add(q0, q0), swap32_add(q1, q1));
        if (lane < 32) s.q[32 * w + lane] = qq;
      }
      if (w == 0 && lane == 0) { s.cntK = 0; s.cntV = 0; s.cntH = 0; s.cntF = 0; s.tag = tag; }
      if (w == 0) TS(12);
    }
    wg_barrier();
    const int rc = sidx * 16 + h;
    const bool reducer = rc < 28;
    const bool ffn1 = X_FFN && sidx == 2 && bh < 56, ffn2 = X_FFN && sidx == 3 && bh < 56;
    const int dpart = bh / 7, dcg = bh - 7 * dpart;
    if (ffn1) {
      const rsrc_t rg = mk_rsrc(w < 2 ? a.wg : a.wu, D * F * 2);
      const int r16 = lane >> 2, vo = 2 * r16 * (F * 2) + (lane & 3) * 16, so = 224 * (w & 1) * (F * 2) + bh * 64;
      u32x4 ga[7], gb[7];
#pragma unroll
      for (int k = 0; k < 7; ++k) { ga[k] = ld_nt(rg, vo, so + 32 * k * (F * 2)); gb[k] = ld_nt(rg, vo, so + (32 * k + 1) * (F * 2)); }
      SB();
      float acc[4][8] = {};
#pragma unroll
      for (int k = 0; k < 7; ++k) mac32(acc, ga[k], gb[k], *(const u32x4*)&s.xn[112 * (w & 1) + 16 * k + r16][0]);
      *(f32x2*)&s.fp[w][2 * lane] = red32w(acc, bit3, lane & 4);
      if (lds_arrive(&s.cntF, lane) == 3) {
        const f32x2 g2 = *(const f32x2*)&s.fp[0][2 * lane] + *(const f32x2*)&s.fp[1][2 * lane];
        const f32x2 u2 = *(const f32x2*)&s.fp[2][2 * lane] + *(const f32x2*)&s.fp[3][2 * lane];
        float hv[2];
#pragma unroll
        for (int e = 0; e < 2; ++e) {
          const float g = rbf(g2[e]), u = rbf(u2[e]);
          const float inner = 0.7978845608028654f * (g + 0.044715f * g * g * g);
          hv[e] = rbf(0.5f * g * (1.f + tanhf(inner))) * u;
        }
        const int pr = (lane & 3) * 4 + ((lane >> 3) & 1) * 2 + ((lane >> 2) & 1);
        __builtin_amdgcn_raw_buffer_store_b64(u32x2{cvtpk(hv[0], hv[1]), tag}, rw, (lane >> 4) * 128 + pr * 8, WS_F + bh * 512, 16);
        TS(14);
      }
    }
    u32x4 da[4], db[4];
    if (ffn2) {
      const rsrc_t rd = mk_rsrc(a.wd, F * D * 2);
      const int vo = 2 * r8 * (D * 2) + c8 * 16, so = (224 * dpart + 16 * w) * (D * 2) + dcg * 128;
#pragma unroll
      for (int m = 0; m < 4; ++m) { const int v_ = m < 3 || w < 2 ? vo : OOB; da[m] = ld_nt(rd, v_, so + 64 * m * (D * 2)); db[m] = ld_nt(rd, v_, so + (64 * m + 1) * (D * 2)); }
    }
    const int r4 = lane >> 4, c16 = lane & 15;
    u32x4 wv[16];
    {
      const rsrc_t ro = mk_rsrc(a.wo + (size_t)(h * DH + 64 * w) * D, 64 * D * 2);
      const int vrow = c16 < 14 ? 2 * r4 * (D * 2) + c16 * 16 : OOB;
#pragma unroll
      for (int i0 = 0; i0 < 16; i0 += WOB) {
#pragma unroll
        for (int i = i0; i < i0 + WOB; ++i) wv[i] = ld_nt(ro, vrow, (8 * (i >> 1) + (i & 1)) * (D * 2) + 224 * sidx);
        SB();
#pragma unroll
        for (int p = i0 / 2; p < (i0 + WOB) / 2; ++p)
#pragma unroll
          for (int e = 0; e < 4; ++e) {
            unsigned lo = perm_lo(wv[2 * p][e], wv[2 * p + 1][e]), hi = perm_hi(wv[2 * p][e], wv[2 * p + 1][e]);
            PIN(lo); PIN(hi);
            wv[2 * p][e] = lo; wv[2 * p + 1][e] = hi;
          }
        SB();
      }
    }
    if (w == 1) TS(17);
    if (ffn2) {
      u32x4 v[1];
      poll<1, 8>(v, rw, w < 3 || lane < 32 ? lane * 16 : OOB, WS_F + dpart * 3584 + w * 1024, 0, tag, w == 3 && lane >= 32);
      if (w < 3 || lane < 32) {
        const int pr = 32 * w + 16 * (lane >> 5) + 2 * (lane & 7), bb = (lane >> 3) & 3;
        s.hT[pr][bb] = v[0][0]; s.hT[pr + 1][bb] = v[0][2];
      }
      lds_signal(&s.cntF, lane);
      lds_wait<1>(&s.cntF, 4);
      if (w == 3) TS(15);
      float acc[4][8] = {};
#pragma unroll
      for (int m = 0; m < 4; ++m) {
        u32x4 x4 = *(const u32x4*)&s.hT[m < 3 || w < 2 ? 32 * m + 8 * w + r8 : 0][0];
        mac32(acc, da[m], db[m], x4);
      }
      float v4[4];
      red32(acc, bit3, v4);
      float* fp = &s.fp[w][(lane >> 4) * 64 + 8 * c8 + (bit3 ? 2 : 0)];
      *(f32x2*)fp = f32x2{v4[0], v4[1]}; *(f32x2*)(fp + 4) = f32x2{v4[2], v4[3]};
      if (lds_arrive(&s.cntF, lane) == 7) {
#pragma unroll
        for (int t2 = 0; t2 < 2; ++t2) {
          const int i = 128 * t2 + 2 * lane;
          const f32x2 y = (*(const f32x2*)&s.fp[0][i] + *(const f32x2*)&s.fp[1][i]) + (*(const f32x2*)&s.fp[2][i] + *(const f32x2*)&s.fp[3][i]);
          st_sc(rw, (lane >> 5) * (28 * 1024) + ((lane >> 3) & 3) * 1024 + c8 * 16, WS_D + (2 * t2 * 28 + 4 * dcg) * 1024 + dpart * 128, y[0], y[1], tag);
        }
        TS(16);
      }
    }
    float y0 = 0.f, y1 = 0.f;
    unsigned xw = 0;
    if (w == 3) {
      if (reducer) {
        xw = __builtin_amdgcn_raw_buffer_load_b32(mk_rsrc(a.x, 4 * D * 2), c8 * 4, (b * D + 16 * rc) * 2, 0);
        u32x4 v[1];
        if (X_FFN) {
          lds_wait<16>(&s.cntK, 4);
          poll<1, 16>(v, rw, lane * 16, WS_D + (b * 28 + rc) * 1024, 0, tag);
          y0 = __uint_as_float(v[0][0]); y1 = __uint_as_float(v[0][2]);
        }
        y0 = rbf(hi3_sum(y0)); y1 = rbf(hi3_sum(y1));
        TS(18);
      }
    }
    if (w == 2) {
      lds_wait<4>(&s.cntK, 4);
      const float mw = s.ml[lane & 3][0], lw = s.ml[lane & 3][1];
      float m = fmaxf(mw, dpp<XOR1>(mw)); m = fmaxf(m, dpp<XOR2>(m));
      float l = lw * __expf(mw - m); l += dpp<XOR1>(l); l += dpp<XOR2>(l);
      st_sc(rw, lane == 0 ? 0 : OOB, WS_M + bh * 64 + sidx * 16, m, l, tag);
      lds_wait<16>(&s.cntK, 8);
      u32x4 v[1];
      poll<1, 8>(v, rw, (lane & 3) * 16, WS_M + bh * 64, 0, tag);
      const float ms = __uint_as_float(v[0][0]);
      float M = fmaxf(ms, dpp<XOR1>(ms)); M = fmaxf(M, dpp<XOR2>(M));
      float L = __uint_as_float(v[0][2]) * __expf(ms - M); L += dpp<XOR1>(L); L += dpp<XOR2>(L);
      if (lane < 4) s.sf[lane] = __expf(mw - M) / L;
      lds_signal(&s.cntV, lane);
      TS(19);
    }
    lds_wait<1>(&s.cntV, 5);
    {
      const int dim = 64 * w + 2 * (4 * (lane & 7) + ((lane >> 3) & 3));
      const f32x4 f = *(const f32x4*)s.sf;
      const f32x2 o = (*(const f32x2*)&s.so[0][dim] * f[0] + *(const f32x2*)&s.so[1][dim] * f[1]) + (*(const f32x2*)&s.so[2][dim] * f[2] + *(const f32x2*)&s.so[3][dim] * f[3]);
      st_sc(rw, lane < 32 ? lane * 16 : OOB, WS_B + id * 2048 + w * 512, o[0], o[1], tag);
      u32x4 v[2];
      poll<2, 0>(v, rw, (lane & 8) * 256 + (r4 * 8 + c8) * 16, WS_B + bh * 8192 + w * 512, 4096, tag);
      if (w == 2) TS(20);
      float oa = __uint_as_float(v[0][0]) + __uint_as_float(v[1][0]), ob = __uint_as_float(v[0][2]) + __uint_as_float(v[1][2]);
      oa += dpp<XOR8>(oa); ob += dpp<XOR8>(ob);
      const unsigned opk = cvtpk(oa, ob);
      float acc[8] = {0, 0, 0, 0, 0, 0, 0, 0};
      static_for<8>([&](auto P) __attribute__((always_inline)) {
        constexpr int p = P.value;
        const unsigned op = __builtin_amdgcn_mov_dpp(opk, 0x150 + p, 0xf, 0xf, true);
#pragma unroll
        for (int e = 0; e < 4; ++e) { acc[2 * e] = dot2(wv[2 * p][e], op, acc[2 * e]); acc[2 * e + 1] = dot2(wv[2 * p + 1][e], op, acc[2 * e + 1]); }
      });
      float u[4];
#pragma unroll
      for (int e = 0; e < 4; ++e) u[e] = swap32_add(acc[e], acc[e + 4]);
      const float y0 = swap16_add(u[0], u[2]), y1 = swap16_add(u[1], u[3]);
      if (c16 < 14) *(f32x2*)&s.sy[w][8 * c16 + 2 * r4] = f32x2{y0, y1};
    }
    FENCE();
    asm volatile("s_waitcnt lgkmcnt(0)" ::: "memory");
    int arrived = 0;
    if (lane == 0) arrived = __hip_atomic_fetch_add(&s.cntH, 1, __ATOMIC_RELAXED, __HIP_MEMORY_SCOPE_WORKGROUP);
    arrived = __builtin_amdgcn_readfirstlane(arrived);
    FENCE();
    if (arrived == 3) {
      const int col = 2 * (lane < 56 ? lane : 0);
      const f32x2 y = (*(const f32x2*)&s.sy[0][col] + *(const f32x2*)&s.sy[1][col]) + (*(const f32x2*)&s.sy[2][col] + *(const f32x2*)&s.sy[3][col]);
      st_sc(rw, lane < 56 ? r8 * 2048 + c8 * 16 : OOB, WS_C + (b * 28 + 7 * sidx) * 2048 + h * 128, y[0], y[1], tag);
      TS(21);
    }
    if (w == 3 && reducer) {
      u32x4 v[2];
      poll<2, 0>(v, rw, lane * 16, WS_C + (b * 28 + rc) * 2048, 1024, tag);
      const float a0 = rbf(hi3_sum(__uint_as_float(v[0][0]) + __uint_as_float(v[1][0]))), a1 = rbf(hi3_sum(__uint_as_float(v[0][2]) + __uint_as_float(v[1][2])));
      const float r0 = rbf(lo_f(xw) + a0) + y0, r1 = rbf(hi_f(xw) + a1) + y1;
      if (lane < 8) a.out[(b * D + 16 * rc) / 2 + lane] = cvtpk(r0, r1);
      if (b == 0 && rc == 0) __builtin_amdgcn_raw_buffer_store_b32(tag, rw, lane == 0 ? 0 : OOB, WS_E, 16);
      TS(22);
    }
  }
}
}

extern "C" void kernel_launch(void* const* d_in, const int* in_sizes, int n_in, void* d_out, int out_size, void* d_ws,
                              size_t ws_size, hipStream_t stream) {
  Args a;
  a.x = (const unsigned short*)d_in[0]; a.K = (const unsigned short*)d_in[1]; a.V = (const unsigned short*)d_in[2];
  a.wq = (const unsigned short*)d_in[3]; a.wo = (const unsigned short*)d_in[4];
  a.wg = (const unsigned short*)d_in[6]; a.wu = (const unsigned short*)d_in[7]; a.wd = (const unsigned short*)d_in[8];
  a.out = (unsigned*)d_out; a.ws = (char*)d_ws;
  k_main<<<256, 512, 0, stream>>>(a);
}
```

```cpp
#include <hip/hip_runtime.h>
#include <hip/hip_bf16.h>
#include <stdint.h>

#define W0 32
#define WV 16
#define TH 32
#define CDIV 2
#define HPRIO 3
#define SPIN_MAX 100000
#define ROTF ((blk >> 1) & 127)
#define X_FFN 1
#define KAUX 2
#define VAUX 2
#define WAUX 0
#define PRET 0
#define WOB 4

namespace {
constexpr int D = 448, NH = 16, DH = 256, F = 1792, HD = NH * DH;
constexpr unsigned WS_E = 0;
constexpr unsigned WS_A = 4096;
constexpr unsigned WS_M = WS_A + 524288;
constexpr unsigned WS_F = WS_M + 4096;
constexpr unsigned WS_D = WS_F + 28672;
constexpr unsigned WS_B = WS_D + 114688;
constexpr unsigned WS_C = WS_B + 524288;
constexpr unsigned WS_END = WS_C + 229376;
constexpr int OOB = 0x40000000;

typedef unsigned u32x4 __attribute__((ext_vector_type(4)));
typedef unsigned u32x2 __attribute__((ext_vector_type(2)));
typedef float f32x4 __attribute__((ext_vector_type(4)));
typedef float f32x2 __attribute__((ext_vector_type(2)));
typedef __bf16 bf16x2 __attribute__((ext_vector_type(2)));
typedef __amdgpu_buffer_rsrc_t rsrc_t;

__device__ __forceinline__ rsrc_t mk_rsrc(const void* p, unsigned bytes) {
  return __builtin_amdgcn_make_buffer_rsrc((void*)p, 0, bytes, 0x00020000);
}
__device__ __forceinline__ u32x4 ld_k(rsrc_t r, int voff, int soff) { return __builtin_amdgcn_raw_buffer_load_b128(r, voff, soff, KAUX); }
__device__ __forceinline__ u32x4 ld_v(rsrc_t r, int voff, int soff) { return __builtin_amdgcn_raw_buffer_load_b128(r, voff, soff, VAUX); }
__device__ __forceinline__ u32x4 ld_w(rsrc_t r, int voff, int soff) { return __builtin_amdgcn_raw_buffer_load_b128(r, voff, soff, WAUX); }
__device__ __forceinline__ u32x4 ld_c(rsrc_t r, int voff, int soff) { return __builtin_amdgcn_raw_buffer_load_b128(r, voff, soff, 0); }
__device__ __forceinline__ u32x4 ld_sc(rsrc_t r, int voff, int soff) { return __builtin_amdgcn_raw_buffer_load_b128(r, voff, soff, 16); }
__device__ __forceinline__ void st_sc(rsrc_t r, int voff, int soff, float a, float b, unsigned tag) {
  __builtin_amdgcn_raw_buffer_store_b128(u32x4{__float_as_uint(a), tag, __float_as_uint(b), tag}, r, voff, soff, 16);
}
__device__ __forceinline__ float lo_f(unsigned w) { return __uint_as_float(w << 16); }
__device__ __forceinline__ float hi_f(unsigned w) { return __uint_as_float(w & 0xffff0000u); }
__device__ __forceinline__ unsigned cvtpk(float lo, float hi) {
  const bf16x2 r = {(__bf16)lo, (__bf16)hi}; return __builtin_bit_cast(unsigned, r);
}
__device__ __forceinline__ float rbf(float x) { return lo_f(cvtpk(x, x)); }
__device__ __forceinline__ float dot2(unsigned a, unsigned b, float acc) {
  return __builtin_amdgcn_fdot2_f32_bf16(__builtin_bit_cast(bf16x2, a), __builtin_bit_cast(bf16x2, b), acc, false);
}
__device__ __forceinline__ unsigned perm_lo(unsigned a, unsigned b) { return __builtin_amdgcn_perm(b, a, 0x05040100u); }
__device__ __forceinline__ unsigned perm_hi(unsigned a, unsigned b) { return __builtin_amdgcn_perm(b, a, 0x07060302u); }
template <int CTRL> __device__ __forceinline__ float dpp(float x) {
  return __builtin_bit_cast(float, __builtin_amdgcn_mov_dpp(__builtin_bit_cast(int, x), CTRL, 0xf, 0xf, true));
}
constexpr int XOR1 = 0xB1, XOR2 = 0x4E, XOR7 = 0x141, XOR8 = 0x128;
__device__ __forceinline__ float swap16_add(float a, float b) {
  auto s = __builtin_amdgcn_permlane16_swap(__float_as_uint(a), __float_as_uint(b), false, false);
  return __uint_as_float(s[0]) + __uint_as_float(s[1]);
}
__device__ __forceinline__ float swap32_add(float a, float b) {
  auto s = __builtin_amdgcn_permlane32_swap(__float_as_uint(a), __float_as_uint(b), false, false);
  return __uint_as_float(s[0]) + __uint_as_float(s[1]);
}
__device__ __forceinline__ float swap16_max(float v) {
  auto s = __builtin_amdgcn_permlane16_swap(__float_as_uint(v), __float_as_uint(v), false, false);
  return fmaxf(__uint_as_float(s[0]), __uint_as_float(s[1]));
}
__device__ __forceinline__ float swap32_max(float v) {
  auto s = __builtin_amdgcn_permlane32_swap(__float_as_uint(v), __float_as_uint(v), false, false);
  return fmaxf(__uint_as_float(s[0]), __uint_as_float(s[1]));
}
template <int CTRL> __device__ __forceinline__ float comb(float a, float b, bool sel) {
  const float t1 = a + dpp<CTRL>(a), t2 = b + dpp<CTRL>(b);
  return sel ? t2 : t1;
}
__device__ __forceinline__ float row_sum(float v) { v += dpp<XOR1>(v); v += dpp<XOR2>(v); v += dpp<XOR7>(v); return v + dpp<XOR8>(v); }
__device__ __forceinline__ float hi3_sum(float v) { v += dpp<XOR8>(v); v = swap16_add(v, v); return swap32_add(v, v); }
__device__ __forceinline__ float hi3_max(float v) { v = fmaxf(v, dpp<XOR8>(v)); return swap32_max(swap16_max(v)); }
__device__ __forceinline__ float wave_sum(float v) { v = row_sum(v); v = swap16_add(v, v); return swap32_add(v, v); }
__device__ __forceinline__ float wave_max(float v) {
  v = fmaxf(v, dpp<XOR1>(v)); v = fmaxf(v, dpp<XOR2>(v)); v = fmaxf(v, dpp<XOR7>(v)); return hi3_max(v);
}
__device__ __forceinline__ void wg_barrier() { asm volatile("s_waitcnt lgkmcnt(0)\n\ts_barrier" ::: "memory"); }
#define SB() __builtin_amdgcn_sched_barrier(0)
#define PIN(x) asm volatile("" : "+v"(x))
#define FENCE() asm volatile("" ::: "memory")
#define TS(slot) do {} while (0)

__device__ __forceinline__ void red32(const float (&acc)[4][8], bool bit3, float (&v)[4]) {
  float u[8];
#pragma unroll
  for (int e = 0; e < 8; ++e) u[e] = swap16_add(swap32_add(acc[0][e], acc[2][e]), swap32_add(acc[1][e], acc[3][e]));
  v[0] = comb<XOR8>(u[0], u[2], bit3); v[1] = comb<XOR8>(u[1], u[3], bit3);
  v[2] = comb<XOR8>(u[4], u[6], bit3); v[3] = comb<XOR8>(u[5], u[7], bit3);
}
__device__ __forceinline__ f32x2 red32w(const float (&acc)[4][8], bool bit3, bool bit2) {
  float u[8], t[4];
#pragma unroll
  for (int e = 0; e < 8; ++e) u[e] = swap16_add(swap32_add(acc[0][e], acc[2][e]), swap32_add(acc[1][e], acc[3][e]));
#pragma unroll
  for (int e = 0; e < 4; ++e) t[e] = comb<XOR8>(u[e], u[e + 4], bit3);
  f32x2 r;
#pragma unroll
  for (int e = 0; e < 2; ++e) {
    int x = __builtin_amdgcn_update_dpp(0, __float_as_int(t[e]), 0x104, 0xf, 0x5, false);
    x = __builtin_amdgcn_update_dpp(x, __float_as_int(t[e + 2]), 0x114, 0xf, 0xa, false);
    r[e] = (bit2 ? t[e + 2] : t[e]) + __int_as_float(x);
  }
  return r;
}
__device__ __forceinline__ void mac32(float (&acc)[4][8], u32x4 A, u32x4 B, u32x4 x4) {
#pragma unroll
  for (int e = 0; e < 4; ++e) {
    const unsigned pl = perm_lo(A[e], B[e]), ph = perm_hi(A[e], B[e]);
#pragma unroll
    for (int bb = 0; bb < 4; ++bb) { acc[bb][2 * e] = dot2(pl, x4[bb], acc[bb][2 * e]); acc[bb][2 * e + 1] = dot2(ph, x4[bb], acc[bb][2 * e + 1]); }
  }
}
template <int N, int SLEEP> __device__ __forceinline__ void poll(u32x4 (&v)[N], rsrc_t r, int voff, int soff, int stride, unsigned tag, bool skip_last = false) {
#pragma unroll 1
  for (int it = 0; it < SPIN_MAX; ++it) {
    FENCE();
#pragma unroll
    for (int i = 0; i < N; ++i) v[i] = ld_sc(r, voff, soff + i * stride);
    bool ok = true;
#pragma unroll
    for (int i = 0; i < N; ++i) { const bool g = v[i][1] == tag && v[i][3] == tag; ok = ok && (g || (i == N - 1 && skip_last)); }
    if (__builtin_amdgcn_ballot_w64(!ok) == 0) break;
    if (SLEEP) __builtin_amdgcn_s_sleep(SLEEP);
  }
}
template <int SLEEP> __device__ __forceinline__ void lds_wait(int* p, int n) {
#pragma unroll 1
  for (int it = 0; it < SPIN_MAX * 64; ++it) {
    FENCE();
    if (__builtin_amdgcn_readfirstlane(__hip_atomic_load(p, __ATOMIC_RELAXED, __HIP_MEMORY_SCOPE_WORKGROUP)) >= n) break;
    if (SLEEP) __builtin_amdgcn_s_sleep(SLEEP);
  }
  FENCE();
}
__device__ __forceinline__ void flag_sync(u32x4* f, int w, int lane, unsigned tag) {
  FENCE();
  if (lane == 0) __hip_atomic_store((unsigned*)f + w, tag, __ATOMIC_RELAXED, __HIP_MEMORY_SCOPE_WORKGROUP);
#pragma unroll 1
  for (int it = 0; it < SPIN_MAX * 64; ++it) {
    FENCE();
    const unsigned v = __hip_atomic_load((unsigned*)f + (lane & 3), __ATOMIC_RELAXED, __HIP_MEMORY_SCOPE_WORKGROUP);
    if (__builtin_amdgcn_ballot_w64(v != tag) == 0) break;
  }
  FENCE();
}
__device__ __forceinline__ int lds_arrive(int* p, int lane) {
  FENCE();
  int n = 0;
  if (lane == 0) n = __hip_atomic_fetch_add(p, 1, __ATOMIC_RELAXED, __HIP_MEMORY_SCOPE_WORKGROUP);
  n = __builtin_amdgcn_readfirstlane(n);
  FENCE();
  return n;
}
__device__ __forceinline__ void lds_signal(int* p, int lane) {
  FENCE();
  if (lane == 0) __hip_atomic_fetch_add(p, 1, __ATOMIC_RELAXED, __HIP_MEMORY_SCOPE_WORKGROUP);
  FENCE();
}

struct Args {
  const unsigned short* x; const unsigned short* K; const unsigned short* V; const unsigned short* wq; const unsigned short* wo;
  const unsigned short* wg; const unsigned short* wu; const unsigned short* wd;
  unsigned* out; char* ws;
};

struct __attribute__((aligned(16))) Lds {
  float sp[4][2][128];
  float so[4][DH];
  float sy[4][112];
  float sf[4];
  unsigned xn[224][4];
  unsigned hT[112][4];
  float fp[4][256];
  f32x4 qp[4][64];
  unsigned q[128];
  u32x4 fl[2];
  float ml[4][2];
  int cntK, cntV, cntH, cntF; unsigned tag;
};

template <int V> struct IC { static constexpr int value = V; };
template <int N, int I = 0, class Fn> __device__ __forceinline__ void static_for(Fn&& f) {
  if constexpr (I < N) { f(IC<I>{}); static_for<N, I + 1>(f); }
}
constexpr int issued(int j) {
  return j < 0 ? W0 : (W0 + (j + 1) / CDIV > j + 1 + WV ? W0 + (j + 1) / CDIV : j + 1 + WV);
}

__global__ void __launch_bounds__(512, 2) k_main(Args a) {
  __shared__ Lds s;
  const int t = threadIdx.x, lane = t & 63, w = __builtin_amdgcn_readfirstlane(t >> 6);
  const int blk = blockIdx.x, xc = blk & 7, r = blk >> 3;
  const int h = 2 * xc + (r & 1), b = r >> 3, sidx = (r >> 1) & 3;
  const int bh = b * NH + h, id = bh * 4 + sidx;
  const rsrc_t rw = mk_rsrc(a.ws, WS_END);

  if (w >= 4) {
    const int ws_ = w - 4;
    const int c = lane & 31, hf = lane >> 5;
    const size_t base = (size_t)id * (1024 * DH) + (size_t)ws_ * 512;
    const unsigned span = 512 * 1024 - ws_ * 1024;
    const rsrc_t rk = mk_rsrc(a.K + base, span), rv = mk_rsrc(a.V + base, span);
    const int rot = ROTF;
    int off = rot * 4096;
    const int vo = lane * 16;
    if (ws_ == 0) TS(0);
    u32x4 kb[128], vb[WV];
    static_for<W0>([&](auto J) __attribute__((always_inline)) {
      constexpr int j = J.value;
      if constexpr (j >= TH) { PIN(kb[j - TH]); SB(); }
      kb[j] = ld_k(rk, vo, off); off = (off + 4096) & 0x7ffff;
      SB();
    });
    if (ws_ == 0) TS(1);
    wg_barrier();
    if (ws_ == 0) TS(2);
    const u32x4 qf = *(const u32x4*)&s.q[4 * c];
    const unsigned tag = s.tag;
    const bool c8 = c & 8, c4 = c & 4, c2 = c & 2, c1 = c & 1;
    float sc[4];
    {
      float p0, p1, p2, p3, p4;
      static_for<128>([&](auto J) __attribute__((always_inline)) {
        constexpr int j = J.value;
        float d = dot2(kb[j][0], qf[0], 0.f); d = dot2(kb[j][1], qf[1], d); d = dot2(kb[j][2], qf[2], d); d = dot2(kb[j][3], qf[3], d);
        PIN(d); SB();
        if constexpr (issued(j) > issued(j - 1)) {
          constexpr int n = issued(j - 1);
          if constexpr (n < 128) kb[n] = ld_k(rk, vo, off); else vb[n - 128] = ld_v(rv, vo, off);
          off = (off + 4096) & 0x7ffff;
        }
        if constexpr (!(j & 1)) p0 = d; else { d = swap16_add(p0, d);
          if constexpr (!(j & 2)) p1 = d; else { d = comb<XOR8>(p1, d, c8);
            if constexpr (!(j & 4)) p2 = d; else { d = comb<XOR7>(p2, d, c4);
              if constexpr (!(j & 8)) p3 = d; else { d = comb<XOR2>(p3, d, c2);
                if constexpr (!(j & 16)) p4 = d; else { d = comb<XOR1>(p4, d, c1); sc[j >> 5] = rbf(d) * 0.0625f; } } } } }
        SB();
      });
    }
    if (ws_ == 0) TS(3);
    const float m = wave_max(fmaxf(fmaxf(sc[0], sc[1]), fmaxf(sc[2], sc[3])));
    const float e0 = __expf(sc[0] - m), e1 = __expf(sc[1] - m), e2 = __expf(sc[2] - m), e3 = __expf(sc[3] - m);
    const int br = __builtin_bitreverse32((unsigned)c) >> 27;
    float* sp = &s.sp[ws_][hf][0];
    sp[br] = e0; sp[32 + br] = e1; sp[64 + br] = e2; sp[96 + br] = e3;
    const float l = wave_sum((e0 + e1) + (e2 + e3));
    if (lane == 0) { s.ml[ws_][0] = m; s.ml[ws_][1] = l; }
    lds_signal(&s.cntK, lane);
    asm volatile("s_waitcnt lgkmcnt(0)" ::: "memory");
    f32x2 acc[4] = {{0, 0}, {0, 0}, {0, 0}, {0, 0}};
    const f32x4* sp4 = (const f32x4*)sp;
    f32x4 pc, pn = sp4[0];
#define VSTEP(j_) { if (!((j_) & 3)) { pc = pn; pn = sp4[(g * (WV / 4) + (j_) / 4 + 1) & 31]; } \
        const u32x4 v = vb[j_]; const float p = pc[(j_) & 3]; const f32x2 pp = {p, p}; \
        _Pragma("unroll") for (int k = 0; k < 4; ++k) { const f32x2 vv = {lo_f(v[k]), hi_f(v[k])}; acc[k] = __builtin_elementwise_fma(vv, pp, acc[k]); PIN(acc[k]); } }
#pragma unroll 1
    for (int g = 0; g < 128 / WV - 1; ++g) {
#pragma unroll
      for (int j = 0; j < WV; ++j) { VSTEP(j); SB(); vb[j] = ld_v(rv, vo, off); off = (off + 4096) & 0x7ffff; SB(); }
      if (g == 64 / WV - 1) lds_signal(&s.cntK, lane);
    }
    {
      const int g = 128 / WV - 1;
#pragma unroll
      for (int j = 0; j < WV; ++j) { VSTEP(j); SB(); }
    }
#undef VSTEP
    {
      const float o0 = swap32_add(acc[0][0], acc[1][0]), o1 = swap32_add(acc[0][1], acc[1][1]);
      const float o2 = swap32_add(acc[2][0], acc[3][0]), o3 = swap32_add(acc[2][1], acc[3][1]);
      float* so = &s.so[ws_][8 * c + 2 * hf];
      *(f32x2*)so = f32x2{o0, o1}; *(f32x2*)(so + 4) = f32x2{o2, o3};
    }
    lds_signal(&s.cntV, lane);
    if (ws_ == 0) TS(5);
  } else {
    if (HPRIO) __builtin_amdgcn_s_setprio(HPRIO);
    const int r8 = lane >> 3, c8 = lane & 7;
    const bool bit3 = lane & 8;
    unsigned tag;
    {
      const unsigned epoch = __builtin_amdgcn_raw_buffer_load_b32(rw, 0, WS_E, 16);
      const u32x4 xr = ld_c(mk_rsrc(a.x + w * D, D * 2), lane * 16, 0);
      const rsrc_t rq = mk_rsrc(a.wq, D * HD * 2);
      u32x4 wa[2], wb[2];
      {
        const int vq = 2 * r8 * (HD * 2) + c8 * 16, vq1 = w < 3 ? vq : OOB, sq = (112 * b + 32 * w) * (HD * 2) + (DH * h + 64 * sidx) * 2;
        wa[0] = ld_w(rq, vq, sq); wb[0] = ld_w(rq, vq, sq + HD * 2);
        wa[1] = ld_w(rq, vq1, sq + 16 * (HD * 2)); wb[1] = ld_w(rq, vq1, sq + 17 * (HD * 2));
      }
      SB();
      {
        f32x2 v[4];
#pragma unroll
        for (int e = 0; e < 4; ++e) v[e] = f32x2{lo_f(xr[e]), hi_f(xr[e])};
        const f32x2 s2 = (v[0] + v[1]) + (v[2] + v[3]);
        const float mean = wave_sum(s2[0] + s2[1]) * (1.f / D);
        f32x2 q2 = {0, 0};
#pragma unroll
        for (int e = 0; e < 4; ++e) { const f32x2 dd = v[e] - mean; q2 = __builtin_elementwise_fma(dd, dd, q2); }
        const float var = rbf(wave_sum(lane < 56 ? q2[0] + q2[1] : 0.f) * (1.f / D));
        const float den = rbf(sqrtf(rbf(var + 1e-5f)));
        const float rinv = __builtin_amdgcn_rcpf(den);
        const float mb = rbf(mean);
        if (lane < 56) {
#pragma unroll
          for (int e = 0; e < 4; ++e) {
            const f32x2 dd = v[e] - mb;
            const unsigned tt = cvtpk(dd[0], dd[1]);
            s.xn[4 * lane + e][w] = cvtpk(lo_f(tt) * rinv, hi_f(tt) * rinv);
          }
        }
      }
      tag = __builtin_amdgcn_readfirstlane(epoch) * 0x9E3779B1u + 0x7F4A7C15u;
      flag_sync(&s.fl[0], w, lane, tag);
      if (w == 0) TS(10);
      {
        float acc[4][8] = {};
        mac32(acc, wa[0], wb[0], *(const u32x4*)&s.xn[56 * b + 16 * w + r8][0]);
        mac32(acc, wa[1], wb[1], *(const u32x4*)&s.xn[56 * b + (w < 3 ? 16 * w + 8 : 48) + r8][0]);
        float v[4];
        red32(acc, bit3, v);
        s.qp[w][lane] = f32x4{v[0], v[1], v[2], v[3]};
      }
      flag_sync(&s.fl[1], w, lane, tag);
      {
        const int L = 16 * w + (lane & 15), k = (lane >> 4) & 1;
        const f32x2* p = (const f32x2*)&s.qp[0][L] + k;
        const f32x2 y = (p[0] + p[128]) + (p[256] + p[384]);
        st_sc(rw, lane < 32 ? c8 * 64 + k * 32 + (bit3 ? 16 : 0) : OOB, WS_A + (h * 4 + w) * 8192 + (sidx * 4 + b) * 512, y[0], y[1], tag);
      }
      if (w == 0) TS(11);
      {
        u32x4 v[2];
        poll<2, 0>(v, rw, lane * 16, WS_A + (h * 4 + b) * 8192 + w * 2048, 1024, tag);
        const float q0 = __uint_as_float(v[0][0]) + __uint_as_float(v[1][0]), q1 = __uint_as_float(v[0][2]) + __uint_as_float(v[1][2]);
        const unsigned qq = cvtpk(swap32_add(q0, q0), swap32_add(q1, q1));
        if (lane < 32) s.q[32 * w + lane] = qq;
      }
      if (w == 0 && lane == 0) { s.cntK = 0; s.cntV = 0; s.cntH = 0; s.cntF = 0; s.tag = tag; }
      if (w == 0) TS(12);
    }
    wg_barrier();
    const int rc = sidx * 16 + h;
    const bool reducer = rc < 28;
    const bool ffn1 = X_FFN && sidx == 2 && bh < 56, ffn2 = X_FFN && sidx == 3 && bh < 56;
    const int dpart = bh / 7, dcg = bh - 7 * dpart;
    if (ffn1) {
      const rsrc_t rg = mk_rsrc(w < 2 ? a.wg : a.wu, D * F * 2);
      const int r16 = lane >> 2, vo = 2 * r16 * (F * 2) + (lane & 3) * 16, so = 224 * (w & 1) * (F * 2) + bh * 64;
      u32x4 ga[7], gb[7];
#pragma unroll
      for (int k = 0; k < 7; ++k) { ga[k] = ld_w(rg, vo, so + 32 * k * (F * 2)); gb[k] = ld_w(rg, vo, so + (32 * k + 1) * (F * 2)); }
      SB();
      float acc[4][8] = {};
#pragma unroll
      for (int k = 0; k < 7; ++k) mac32(acc, ga[k], gb[k], *(const u32x4*)&s.xn[112 * (w & 1) + 16 * k + r16][0]);
      *(f32x2*)&s.fp[w][2 * lane] = red32w(acc, bit3, lane & 4);
      if (lds_arrive(&s.cntF, lane) == 3) {
        const f32x2 g2 = *(const f32x2*)&s.fp[0][2 * lane] + *(const f32x2*)&s.fp[1][2 * lane];
        const f32x2 u2 = *(const f32x2*)&s.fp[2][2 * lane] + *(const f32x2*)&s.fp[3][2 * lane];
        float hv[2];
#pragma unroll
        for (int e = 0; e < 2; ++e) {
          const float g = rbf(g2[e]), u = rbf(u2[e]);
          const float inner = 0.7978845608028654f * (g + 0.044715f * g * g * g);
          hv[e] = rbf(0.5f * g * (1.f + tanhf(inner))) * u;
        }
        const int pr = (lane & 3) * 4 + ((lane >> 3) & 1) * 2 + ((lane >> 2) & 1);
        __builtin_amdgcn_raw_buffer_store_b64(u32x2{cvtpk(hv[0], hv[1]), tag}, rw, (lane >> 4) * 128 + pr * 8, WS_F + bh * 512, 16);
        TS(14);
      }
    }
    u32x4 da[4], db[4];
    if (ffn2) {
      const rsrc_t rd = mk_rsrc(a.wd, F * D * 2);
      const int vo = 2 * r8 * (D * 2) + c8 * 16, so = (224 * dpart + 16 * w) * (D * 2) + dcg * 128;
#pragma unroll
      for (int m = 0; m < 4; ++m) { const int v_ = m < 3 || w < 2 ? vo : OOB; da[m] = ld_w(rd, v_, so + 64 * m * (D * 2)); db[m] = ld_w(rd, v_, so + (64 * m + 1) * (D * 2)); }
    }
    const int r4 = lane >> 4, c16 = lane & 15;
    u32x4 wv[16];
    {
      const rsrc_t ro = mk_rsrc(a.wo + (size_t)(h * DH + 64 * w) * D, 64 * D * 2);
      const int vrow = c16 < 14 ? 2 * r4 * (D * 2) + c16 * 16 : OOB;
#pragma unroll
      for (int i0 = 0; i0 < 16; i0 += WOB) {
#pragma unroll
        for (int i = i0; i < i0 + WOB; ++i) wv[i] = ld_w(ro, vrow, (8 * (i >> 1) + (i & 1)) * (D * 2) + 224 * sidx);
        SB();
#pragma unroll
        for (int p = i0 / 2; p < (i0 + WOB) / 2; ++p)
#pragma unroll
          for (int e = 0; e < 4; ++e) {
            unsigned lo = perm_lo(wv[2 * p][e], wv[2 * p + 1][e]), hi = perm_hi(wv[2 * p][e], wv[2 * p + 1][e]);
            PIN(lo); PIN(hi);
            wv[2 * p][e] = lo; wv[2 * p + 1][e] = hi;
          }
        SB();
      }
    }
    if (w == 1) TS(17);
    if (ffn2) {
      u32x4 v[1];
      poll<1, 8>(v, rw, w < 3 || lane < 32 ? lane * 16 : OOB, WS_F + dpart * 3584 + w * 1024, 0, tag, w == 3 && lane >= 32);
      if (w < 3 || lane < 32) {
        const int pr = 32 * w + 16 * (lane >> 5) + 2 * (lane & 7), bb = (lane >> 3) & 3;
        s.hT[pr][bb] = v[0][0]; s.hT[pr + 1][bb] = v[0][2];
      }
      lds_signal(&s.cntF, lane);
      lds_wait<1>(&s.cntF, 4);
      if (w == 3) TS(15);
      float acc[4][8] = {};
#pragma unroll
      for (int m = 0; m < 4; ++m) {
        u32x4 x4 = *(const u32x4*)&s.hT[m < 3 || w < 2 ? 32 * m + 8 * w + r8 : 0][0];
        mac32(acc, da[m], db[m], x4);
      }
      float v4[4];
      red32(acc, bit3, v4);
      float* fp = &s.fp[w][(lane >> 4) * 64 + 8 * c8 + (bit3 ? 2 : 0)];
      *(f32x2*)fp = f32x2{v4[0], v4[1]}; *(f32x2*)(fp + 4) = f32x2{v4[2], v4[3]};
      if (lds_arrive(&s.cntF, lane) == 7) {
#pragma unroll
        for (int t2 = 0; t2 < 2; ++t2) {
          const int i = 128 * t2 + 2 * lane;
          const f32x2 y = (*(const f32x2*)&s.fp[0][i] + *(const f32x2*)&s.fp[1][i]) + (*(const f32x2*)&s.fp[2][i] + *(const f32x2*)&s.fp[3][i]);
          st_sc(rw, (lane >> 5) * (28 * 1024) + ((lane >> 3) & 3) * 1024 + c8 * 16, WS_D + (2 * t2 * 28 + 4 * dcg) * 1024 + dpart * 128, y[0], y[1], tag);
        }
        TS(16);
      }
    }
    float y0 = 0.f, y1 = 0.f;
    unsigned xw = 0;
    if (w == 3) {
      if (reducer) {
        xw = __builtin_amdgcn_raw_buffer_load_b32(mk_rsrc(a.x, 4 * D * 2), c8 * 4, (b * D + 16 * rc) * 2, 0);
        u32x4 v[1];
        if (X_FFN) {
          lds_wait<16>(&s.cntK, 4);
          poll<1, 16>(v, rw, lane * 16, WS_D + (b * 28 + rc) * 1024, 0, tag);
          y0 = __uint_as_float(v[0][0]); y1 = __uint_as_float(v[0][2]);
        }
        y0 = rbf(hi3_sum(y0)); y1 = rbf(hi3_sum(y1));
        TS(18);
      }
    }
    if (w == 2) {
      lds_wait<4>(&s.cntK, 4);
      const float mw = s.ml[lane & 3][0], lw = s.ml[lane & 3][1];
      float m = fmaxf(mw, dpp<XOR1>(mw)); m = fmaxf(m, dpp<XOR2>(m));
      float l = lw * __expf(mw - m); l += dpp<XOR1>(l); l += dpp<XOR2>(l);
      st_sc(rw, lane == 0 ? 0 : OOB, WS_M + bh * 64 + sidx * 16, m, l, tag);
      lds_wait<16>(&s.cntK, 8);
      u32x4 v[1];
      poll<1, 8>(v, rw, (lane & 3) * 16, WS_M + bh * 64, 0, tag);
      const float ms = __uint_as_float(v[0][0]);
      float M = fmaxf(ms, dpp<XOR1>(ms)); M = fmaxf(M, dpp<XOR2>(M));
      float L = __uint_as_float(v[0][2]) * __expf(ms - M); L += dpp<XOR1>(L); L += dpp<XOR2>(L);
      if (lane < 4) s.sf[lane] = __expf(mw - M) / L;
      lds_signal(&s.cntV, lane);
      TS(19);
    }
    lds_wait<1>(&s.cntV, 5);
    {
      const int dim = 64 * w + 2 * (4 * (lane & 7) + ((lane >> 3) & 3));
      const f32x4 f = *(const f32x4*)s.sf;
      const f32x2 o = (*(const f32x2*)&s.so[0][dim] * f[0] + *(const f32x2*)&s.so[1][dim] * f[1]) + (*(const f32x2*)&s.so[2][dim] * f[2] + *(const f32x2*)&s.so[3][dim] * f[3]);
      st_sc(rw, lane < 32 ? lane * 16 : OOB, WS_B + id * 2048 + w * 512, o[0], o[1], tag);
      u32x4 v[2];
      poll<2, 0>(v, rw, (lane & 8) * 256 + (r4 * 8 + c8) * 16, WS_B + bh * 8192 + w * 512, 4096, tag);
      if (w == 2) TS(20);
      float oa = __uint_as_float(v[0][0]) + __uint_as_float(v[1][0]), ob = __uint_as_float(v[0][2]) + __uint_as_float(v[1][2]);
      oa += dpp<XOR8>(oa); ob += dpp<XOR8>(ob);
      const unsigned opk = cvtpk(oa, ob);
      float acc[8] = {0, 0, 0, 0, 0, 0, 0, 0};
      static_for<8>([&](auto P) __attribute__((always_inline)) {
        constexpr int p = P.value;
        const unsigned op = __builtin_amdgcn_mov_dpp(opk, 0x150 + p, 0xf, 0xf, true);
#pragma unroll
        for (int e = 0; e < 4; ++e) { acc[2 * e] = dot2(wv[2 * p][e], op, acc[2 * e]); acc[2 * e + 1] = dot2(wv[2 * p + 1][e], op, acc[2 * e + 1]); }
      });
      float u[4];
#pragma unroll
      for (int e = 0; e < 4; ++e) u[e] = swap32_add(acc[e], acc[e + 4]);
      const float y0 = swap16_add(u[0], u[2]), y1 = swap16_add(u[1], u[3]);
      if (c16 < 14) *(f32x2*)&s.sy[w][8 * c16 + 2 * r4] = f32x2{y0, y1};
    }
    FENCE();
    asm volatile("s_waitcnt lgkmcnt(0)" ::: "memory");
    int arrived = 0;
    if (lane == 0) arrived = __hip_atomic_fetch_add(&s.cntH, 1, __ATOMIC_RELAXED, __HIP_MEMORY_SCOPE_WORKGROUP);
    arrived = __builtin_amdgcn_readfirstlane(arrived);
    FENCE();
    if (arrived == 3) {
      const int col = 2 * (lane < 56 ? lane : 0);
      const f32x2 y = (*(const f32x2*)&s.sy[0][col] + *(const f32x2*)&s.sy[1][col]) + (*(const f32x2*)&s.sy[2][col] + *(const f32x2*)&s.sy[3][col]);
      st_sc(rw, lane < 56 ? r8 * 2048 + c8 * 16 : OOB, WS_C + (b * 28 + 7 * sidx) * 2048 + h * 128, y[0], y[1], tag);
      TS(21);
    }
    if (w == 3 && reducer) {
      u32x4 v[2];
      poll<2, 0>(v, rw, lane * 16, WS_C + (b * 28 + rc) * 2048, 1024, tag);
      const float a0 = rbf(hi3_sum(__uint_as_float(v[0][0]) + __uint_as_float(v[1][0]))), a1 = rbf(hi3_sum(__uint_as_float(v[0][2]) + __uint_as_float(v[1][2])));
      const float r0 = rbf(lo_f(xw) + a0) + y0, r1 = rbf(hi_f(xw) + a1) + y1;
      if (lane < 8) a.out[(b * D + 16 * rc) / 2 + lane] = cvtpk(r0, r1);
      if (b == 0 && rc == 0) __builtin_amdgcn_raw_buffer_store_b32(tag, rw, lane == 0 ? 0 : OOB, WS_E, 16);
      TS(22);
    }
  }
}
}

extern "C" void kernel_launch(void* const* d_in, const int* in_sizes, int n_in, void* d_out, int out_size, void* d_ws,
                              size_t ws_size, hipStream_t stream) {
  Args a;
  a.x = (const unsigned short*)d_in[0]; a.K = (const unsigned short*)d_in[1]; a.V = (const unsigned short*)d_in[2];
  a.wq = (const unsigned short*)d_in[3]; a.wo = (const unsigned short*)d_in[4];
  a.wg = (const unsigned short*)d_in[6]; a.wu = (const unsigned short*)d_in[7]; a.wd = (const unsigned short*)d_in[8];
  a.out = (unsigned*)d_out; a.ws = (char*)d_ws;
  k_main<<<256, 512, 0, stream>>>(a);
}
```

```cpp
#include <hip/hip_runtime.h>
#include <hip/hip_bf16.h>
#include <stdint.h>

#define W0 32
#define WV 16
#define TH 32
#define CDIV 2
#define HPRIO 3
#define SPIN_MAX 100000
#define ROTF ((blk >> 1) & 127)
#define X_FFN 1
#define KAUX 2
#define VAUX 2
#define WAUX 0
#define PRET 0
#define APS 0
#define SDEL 0
#define X_WO 1
#define X_Q 1
#define CPF 0
#define CPFW 7
#define EARLYE 0
#define OUTAUX 0
#define PCK 0
#define PCV 0
#define PA 0
#define PK 1950
#define PV 1800
#define PINC 0
#define PGAP 4
#define WOB 4

namespace {
constexpr int D = 448, NH = 16, DH = 256, F = 1792, HD = NH * DH;
constexpr unsigned WS_E = 0;
constexpr unsigned WS_A = 4096;
constexpr unsigned WS_M = WS_A + 65536;
constexpr unsigned WS_F = WS_M + 4096;
constexpr unsigned WS_D = WS_F + 57344;
constexpr unsigned WS_B = WS_D + 114688;
constexpr unsigned WS_C = WS_B + 524288;
constexpr unsigned WS_END = WS_C + 229376;
constexpr int OOB = 0x40000000;

typedef unsigned u32x4 __attribute__((ext_vector_type(4)));
typedef unsigned u32x2 __attribute__((ext_vector_type(2)));
typedef float f32x4 __attribute__((ext_vector_type(4)));
typedef float f32x2 __attribute__((ext_vector_type(2)));
typedef __bf16 bf16x2 __attribute__((ext_vector_type(2)));
typedef __amdgpu_buffer_rsrc_t rsrc_t;

__device__ __forceinline__ rsrc_t mk_rsrc(const void* p, unsigned bytes) {
  return __builtin_amdgcn_make_buffer_rsrc((void*)p, 0, bytes, 0x00020000);
}
__device__ __forceinline__ u32x4 ld_k(rsrc_t r, int voff, int soff) { return __builtin_amdgcn_raw_buffer_load_b128(r, voff, soff, KAUX); }
__device__ __forceinline__ u32x4 ld_v(rsrc_t r, int voff, int soff) { return __builtin_amdgcn_raw_buffer_load_b128(r, voff, soff, VAUX); }
__device__ __forceinline__ u32x4 ld_w(rsrc_t r, int voff, int soff) { return __builtin_amdgcn_raw_buffer_load_b128(r, voff, soff, WAUX); }
__device__ __forceinline__ u32x4 ld_c(rsrc_t r, int voff, int soff) { return __builtin_amdgcn_raw_buffer_load_b128(r, voff, soff, 0); }
__device__ __forceinline__ u32x4 ld_sc(rsrc_t r, int voff, int soff) { return __builtin_amdgcn_raw_buffer_load_b128(r, voff, soff, 16); }
__device__ __forceinline__ void st_sc(rsrc_t r, int voff, int soff, float a, float b, unsigned tag) {
  __builtin_amdgcn_raw_buffer_store_b128(u32x4{__float_as_uint(a), tag, __float_as_uint(b), tag}, r, voff, soff, 16);
}
__device__ __forceinline__ void st_sc2(rsrc_t r, int voff, int soff, unsigned a, unsigned b, unsigned tag) {
  __builtin_amdgcn_raw_buffer_store_b128(u32x4{a, tag, b, tag}, r, voff, soff, 16);
}
__device__ __forceinline__ float lo_f(unsigned w) { return __uint_as_float(w << 16); }
__device__ __forceinline__ float hi_f(unsigned w) { return __uint_as_float(w & 0xffff0000u); }
__device__ __forceinline__ unsigned cvtpk(float lo, float hi) {
  const bf16x2 r = {(__bf16)lo, (__bf16)hi}; return __builtin_bit_cast(unsigned, r);
}
__device__ __forceinline__ float rbf(float x) { return lo_f(cvtpk(x, x)); }
__device__ __forceinline__ float dot2(unsigned a, unsigned b, float acc) {
  return __builtin_amdgcn_fdot2_f32_bf16(__builtin_bit_cast(bf16x2, a), __builtin_bit_cast(bf16x2, b), acc, false);
}
__device__ __forceinline__ unsigned perm_lo(unsigned a, unsigned b) { return __builtin_amdgcn_perm(b, a, 0x05040100u); }
__device__ __forceinline__ unsigned perm_hi(unsigned a, unsigned b) { return __builtin_amdgcn_perm(b, a, 0x07060302u); }
template <int CTRL> __device__ __forceinline__ float dpp(float x) {
  return __builtin_bit_cast(float, __builtin_amdgcn_mov_dpp(__builtin_bit_cast(int, x), CTRL, 0xf, 0xf, true));
}
constexpr int XOR1 = 0xB1, XOR2 = 0x4E, XOR7 = 0x141, XOR8 = 0x128;
__device__ __forceinline__ float swap16_add(float a, float b) {
  auto s = __builtin_amdgcn_permlane16_swap(__float_as_uint(a), __float_as_uint(b), false, false);
  return __uint_as_float(s[0]) + __uint_as_float(s[1]);
}
__device__ __forceinline__ float swap32_add(float a, float b) {
  auto s = __builtin_amdgcn_permlane32_swap(__float_as_uint(a), __float_as_uint(b), false, false);
  return __uint_as_float(s[0]) + __uint_as_float(s[1]);
}
__device__ __forceinline__ float swap16_max(float v) {
  auto s = __builtin_amdgcn_permlane16_swap(__float_as_uint(v), __float_as_uint(v), false, false);
  return fmaxf(__uint_as_float(s[0]), __uint_as_float(s[1]));
}
__device__ __forceinline__ float swap32_max(float v) {
  auto s = __builtin_amdgcn_permlane32_swap(__float_as_uint(v), __float_as_uint(v), false, false);
  return fmaxf(__uint_as_float(s[0]), __uint_as_float(s[1]));
}
template <int CTRL> __device__ __forceinline__ float comb(float a, float b, bool sel) {
  const float t1 = a + dpp<CTRL>(a), t2 = b + dpp<CTRL>(b);
  return sel ? t2 : t1;
}
__device__ __forceinline__ float row_sum(float v) { v += dpp<XOR1>(v); v += dpp<XOR2>(v); v += dpp<XOR7>(v); return v + dpp<XOR8>(v); }
__device__ __forceinline__ float hi3_sum(float v) { v += dpp<XOR8>(v); v = swap16_add(v, v); return swap32_add(v, v); }
__device__ __forceinline__ float hi3_max(float v) { v = fmaxf(v, dpp<XOR8>(v)); return swap32_max(swap16_max(v)); }
__device__ __forceinline__ float wave_sum(float v) { v = row_sum(v); v = swap16_add(v, v); return swap32_add(v, v); }
__device__ __forceinline__ float wave_max(float v) {
  v = fmaxf(v, dpp<XOR1>(v)); v = fmaxf(v, dpp<XOR2>(v)); v = fmaxf(v, dpp<XOR7>(v)); return hi3_max(v);
}
__device__ __forceinline__ void wg_barrier() { asm volatile("s_waitcnt lgkmcnt(0)\n\ts_barrier" ::: "memory"); }
#define SB() __builtin_amdgcn_sched_barrier(0)
#define PIN(x) asm volatile("" : "+v"(x))
#define FENCE() asm volatile("" ::: "memory")
#define TS(slot) do {} while (0)

__device__ __forceinline__ void red32(const float (&acc)[4][8], bool bit3, float (&v)[4]) {
  float u[8];
#pragma unroll
  for (int e = 0; e < 8; ++e) u[e] = swap16_add(swap32_add(acc[0][e], acc[2][e]), swap32_add(acc[1][e], acc[3][e]));
  v[0] = comb<XOR8>(u[0], u[2], bit3); v[1] = comb<XOR8>(u[1], u[3], bit3);
  v[2] = comb<XOR8>(u[4], u[6], bit3); v[3] = comb<XOR8>(u[5], u[7], bit3);
}
__device__ __forceinline__ f32x4 red32c(const float (&acc)[4][8], bool bit3) {
  float u[8];
#pragma unroll
  for (int e = 0; e < 8; ++e) u[e] = swap16_add(swap32_add(acc[0][e], acc[2][e]), swap32_add(acc[1][e], acc[3][e]));
  return f32x4{comb<XOR8>(u[0], u[4], bit3), comb<XOR8>(u[1], u[5], bit3), comb<XOR8>(u[2], u[6], bit3), comb<XOR8>(u[3], u[7], bit3)};
}
__device__ __forceinline__ void mac32(float (&acc)[4][8], u32x4 A, u32x4 B, u32x4 x4) {
#pragma unroll
  for (int e = 0; e < 4; ++e) {
    const unsigned pl = perm_lo(A[e], B[e]), ph = perm_hi(A[e], B[e]);
#pragma unroll
    for (int bb = 0; bb < 4; ++bb) { acc[bb][2 * e] = dot2(pl, x4[bb], acc[bb][2 * e]); acc[bb][2 * e + 1] = dot2(ph, x4[bb], acc[bb][2 * e + 1]); }
  }
}
template <int N, int SLEEP> __device__ __forceinline__ void poll(u32x4 (&v)[N], rsrc_t r, int voff, int soff, int stride, unsigned tag, bool skip_last = false, bool skip_all = false) {
#pragma unroll 1
  for (int it = 0; it < SPIN_MAX; ++it) {
    FENCE();
#pragma unroll
    for (int i = 0; i < N; ++i) v[i] = ld_sc(r, voff, soff + i * stride);
    bool ok = true;
#pragma unroll
    for (int i = 0; i < N; ++i) { const bool g = v[i][1] == tag && v[i][3] == tag; ok = ok && (g || skip_all || (i == N - 1 && skip_last)); }
    if (__builtin_amdgcn_ballot_w64(!ok) == 0) break;
    if (SLEEP) __builtin_amdgcn_s_sleep(SLEEP);
  }
}
template <int MODE> __device__ __forceinline__ void poll2(u32x4 (&v)[2], u32x4 (&fl)[4], rsrc_t r, int voff, int soff, int stride, unsigned tag) {
  if constexpr (MODE == 0) { poll<2, 0>(v, r, voff, soff, stride, tag); return; }
  int o0 = voff, o1 = voff;
  unsigned a0 = 0, a1 = 0, b0 = 0, b1 = 0;
  auto take = [&](u32x4 x, u32x4 y) __attribute__((always_inline)) {
    const bool gx = x[1] == tag && x[3] == tag, gy = y[1] == tag && y[3] == tag;
    a0 = gx ? x[0] : a0; a1 = gx ? x[2] : a1; o0 = gx ? OOB : o0;
    b0 = gy ? y[0] : b0; b1 = gy ? y[2] : b1; o1 = gy ? OOB : o1;
    return __builtin_amdgcn_ballot_w64((o0 & o1) != OOB) == 0;
  };
  if constexpr (MODE == 1) {
#pragma unroll 1
    for (int it = 0; it < SPIN_MAX; ++it) {
      FENCE();
      const u32x4 x = ld_sc(r, o0, soff), y = ld_sc(r, o1, soff + stride);
      if (take(x, y)) break;
    }
  } else {
    FENCE();
    fl[0] = ld_sc(r, o0, soff); fl[1] = ld_sc(r, o1, soff + stride);
    fl[2] = fl[3] = u32x4{0, 0, 0, 0};
#pragma unroll 1
    for (int it = 0; it < SPIN_MAX; ++it) {
      __builtin_amdgcn_s_sleep(PGAP);
      FENCE();
      fl[2] = ld_sc(r, o0, soff); fl[3] = ld_sc(r, o1, soff + stride);
      SB();
      if (take(fl[0], fl[1])) break;
      __builtin_amdgcn_s_sleep(PGAP);
      FENCE();
      fl[0] = ld_sc(r, o0, soff); fl[1] = ld_sc(r, o1, soff + stride);
      SB();
      if (take(fl[2], fl[3])) break;
    }
  }
  v[0][0] = a0; v[0][2] = a1; v[1][0] = b0; v[1][2] = b1;
}
template <int SLEEP> __device__ __forceinline__ void lds_wait(int* p, int n) {
#pragma unroll 1
  for (int it = 0; it < SPIN_MAX * 64; ++it) {
    FENCE();
    if (__builtin_amdgcn_readfirstlane(__hip_atomic_load(p, __ATOMIC_RELAXED, __HIP_MEMORY_SCOPE_WORKGROUP)) >= n) break;
    if (SLEEP) __builtin_amdgcn_s_sleep(SLEEP);
  }
  FENCE();
}
__device__ __forceinline__ int lds_arrive(int* p, int lane) {
  FENCE();
  int n = 0;
  if (lane == 0) n = __hip_atomic_fetch_add(p, 1, __ATOMIC_RELAXED, __HIP_MEMORY_SCOPE_WORKGROUP);
  n = __builtin_amdgcn_readfirstlane(n);
  FENCE();
  return n;
}
__device__ __forceinline__ void lds_signal(int* p, int lane) {
  FENCE();
  if (lane == 0) __hip_atomic_fetch_add(p, 1, __ATOMIC_RELAXED, __HIP_MEMORY_SCOPE_WORKGROUP);
  FENCE();
}

__device__ __forceinline__ void pace(unsigned target) {
#pragma unroll 1
  for (int it = 0; it < 4000; ++it) {
    if ((int)((unsigned)__builtin_amdgcn_s_memrealtime() - target) >= 0) break;
    __builtin_amdgcn_s_sleep(1);
  }
}

struct Args {
  const unsigned short* x; const unsigned short* K; const unsigned short* V; const unsigned short* wq; const unsigned short* wo;
  const unsigned short* wg; const unsigned short* wu; const unsigned short* wd;
  unsigned* out; char* ws;
};

enum { CNT_K, CNT_V, CNT_H, CNT_F, CNT_X, CNT_Q };
struct __attribute__((aligned(16))) Lds {
  float sp[4][2][128];
  float so[4][DH];
  float sy[4][192];
  float sf[4];
  unsigned xn[224][4];
  unsigned hT[112][4];
  float fp[4][256];
  f32x4 qp[4][64];
  unsigned q[128];
  float ml[4][2];
  int cnt[8];
};

template <int V> struct IC { static constexpr int value = V; };
template <int N, int I = 0, class Fn> __device__ __forceinline__ void static_for(Fn&& f) {
  if constexpr (I < N) { f(IC<I>{}); static_for<N, I + 1>(f); }
}
constexpr int issued(int j) {
  return j < 0 ? W0 : (W0 + (j + 1) / CDIV > j + 1 + WV ? W0 + (j + 1) / CDIV : j + 1 + WV);
}

__global__ void __launch_bounds__(512, 2) k_main(Args a) {
  __shared__ Lds s;
  const int t = threadIdx.x, lane = t & 63, w = __builtin_amdgcn_readfirstlane(t >> 6);
  const int blk = blockIdx.x, xc = blk & 7, r = blk >> 3;
  const int h = 2 * xc + (r & 1), b = r >> 3, sidx = (r >> 1) & 3;
  const int bh = b * NH + h, id = bh * 4 + sidx;
  const rsrc_t rw = mk_rsrc(a.ws, WS_END);
  if (t < 8) s.cnt[t] = 0;

  if (w >= 4) {
    const int ws_ = w - 4;
    const int c = lane & 31, hf = lane >> 5;
    const size_t base = (size_t)id * (1024 * DH) + (size_t)ws_ * 512;
    const unsigned span = 512 * 1024 - ws_ * 1024;
    const rsrc_t rk = mk_rsrc(a.K + base, span), rv = mk_rsrc(a.V + base, span);
    const int rot = ROTF;
    int off = rot * 4096;
    const int vo = lane * 16;
    if (ws_ == 0) TS(0);
    wg_barrier();
    const unsigned t0 = (PCK + PCV != 0) ? (unsigned)__builtin_amdgcn_s_memrealtime() + PA : 0;
    if (SDEL && sidx == 0) __builtin_amdgcn_s_sleep(SDEL);
    u32x4 kb[128], vb[WV];
    static_for<W0>([&](auto J) __attribute__((always_inline)) {
      constexpr int j = J.value;
      if constexpr (j >= TH) { PIN(kb[j - TH]); SB(); }
      kb[j] = ld_k(rk, vo, off); off = (off + 4096) & 0x7ffff;
      SB();
    });
    if (ws_ == 0) TS(1);
    wg_barrier();
    if (ws_ == 0) TS(2);
    const u32x4 qf = *(const u32x4*)&s.q[4 * c];
    const bool c8 = c & 8, c4 = c & 4, c2 = c & 2, c1 = c & 1;
    float sc[4];
    {
      float p0, p1, p2, p3, p4;
      static_for<128>([&](auto J) __attribute__((always_inline)) {
        constexpr int j = J.value;
        float d = dot2(kb[j][0], qf[0], 0.f); d = dot2(kb[j][1], qf[1], d); d = dot2(kb[j][2], qf[2], d); d = dot2(kb[j][3], qf[3], d);
        PIN(d); SB();
        if constexpr (issued(j) > issued(j - 1)) {
          constexpr int n = issued(j - 1);
          if constexpr (PCK && n % (PCK ? PCK : 1) == 0) pace(t0 + (n < 128 ? n * PK / 128 : PK + (n - 128) * PV / 128));
          if constexpr (n < 128) kb[n] = ld_k(rk, vo, off); else vb[n - 128] = ld_v(rv, vo, off);
          off = (off + 4096) & 0x7ffff;
        }
        if constexpr (!(j & 1)) p0 = d; else { d = swap16_add(p0, d);
          if constexpr (!(j & 2)) p1 = d; else { d = comb<XOR8>(p1, d, c8);
            if constexpr (!(j & 4)) p2 = d; else { d = comb<XOR7>(p2, d, c4);
              if constexpr (!(j & 8)) p3 = d; else { d = comb<XOR2>(p3, d, c2);
                if constexpr (!(j & 16)) p4 = d; else { d = comb<XOR1>(p4, d, c1); sc[j >> 5] = rbf(d) * 0.0625f; } } } } }
        SB();
      });
    }
    if (ws_ == 0) TS(3);
    const float m = wave_max(fmaxf(fmaxf(sc[0], sc[1]), fmaxf(sc[2], sc[3])));
    const float e0 = __expf(sc[0] - m), e1 = __expf(sc[1] - m), e2 = __expf(sc[2] - m), e3 = __expf(sc[3] - m);
    const int br = __builtin_bitreverse32((unsigned)c) >> 27;
    float* sp = &s.sp[ws_][hf][0];
    sp[br] = e0; sp[32 + br] = e1; sp[64 + br] = e2; sp[96 + br] = e3;
    const float l = wave_sum((e0 + e1) + (e2 + e3));
    if (lane == 0) { s.ml[ws_][0] = m; s.ml[ws_][1] = l; }
    lds_signal(&s.cnt[CNT_K], lane);
    asm volatile("s_waitcnt lgkmcnt(0)" ::: "memory");
    f32x2 acc[4] = {{0, 0}, {0, 0}, {0, 0}, {0, 0}};
    const f32x4* sp4 = (const f32x4*)sp;
    f32x4 pc, pn = sp4[0];
#define VSTEP(j_) { if (!((j_) & 3)) { pc = pn; pn = sp4[(g * (WV / 4) + (j_) / 4 + 1) & 31]; } \
        const u32x4 v = vb[j_]; const float p = pc[(j_) & 3]; const f32x2 pp = {p, p}; \
        _Pragma("unroll") for (int k = 0; k < 4; ++k) { const f32x2 vv = {lo_f(v[k]), hi_f(v[k])}; acc[k] = __builtin_elementwise_fma(vv, pp, acc[k]); PIN(acc[k]); } }
#pragma unroll 1
    for (int g = 0; g < 128 / WV - 1; ++g) {
#pragma unroll
      for (int j = 0; j < WV; ++j) {
        VSTEP(j); SB();
        if (PCV && j % (PCV ? PCV : 1) == 0) pace(t0 + PK + (((g + 1) * WV + j) * PV) / 128);
        vb[j] = ld_v(rv, vo, off); off = (off + 4096) & 0x7ffff; SB();
      }
      if (g == 64 / WV - 1) lds_signal(&s.cnt[CNT_K], lane);
    }
    {
      const int g = 128 / WV - 1;
#pragma unroll
      for (int j = 0; j < WV; ++j) { VSTEP(j); SB(); }
    }
#undef VSTEP
    {
      const float o0 = swap32_add(acc[0][0], acc[1][0]), o1 = swap32_add(acc[0][1], acc[1][1]);
      const float o2 = swap32_add(acc[2][0], acc[3][0]), o3 = swap32_add(acc[2][1], acc[3][1]);
      float* so = &s.so[ws_][8 * c + 2 * hf];
      *(f32x2*)so = f32x2{o0, o1}; *(f32x2*)(so + 4) = f32x2{o2, o3};
    }
    lds_signal(&s.cnt[CNT_V], lane);
    if (ws_ == 0) TS(5);
  } else {
    if (HPRIO) __builtin_amdgcn_s_setprio(HPRIO);
    const int r8 = lane >> 3, c8 = lane & 7;
    const bool bit3 = lane & 8;
    u32x4 pa[7], pb[7];
    auto proj_load = [&](rsrc_t r, int stride, int col) __attribute__((always_inline)) {
      const int vo = 2 * r8 * stride + c8 * 16, so = 112 * w * stride + col;
#pragma unroll
      for (int k = 0; k < 7; ++k) { pa[k] = ld_w(r, vo, so + 16 * k * stride); pb[k] = ld_w(r, vo, so + (16 * k + 1) * stride); }
    };
    auto proj_mac = [&]() __attribute__((always_inline)) {
      float acc[4][8] = {};
#pragma unroll
      for (int k = 0; k < 7; ++k) mac32(acc, pa[k], pb[k], *(const u32x4*)&s.xn[56 * w + 8 * k + r8][0]);
      s.qp[w][lane] = red32c(acc, bit3);
    };
    auto proj_sum = [&]() __attribute__((always_inline)) {
      const f32x4 y = (s.qp[0][lane] + s.qp[1][lane]) + (s.qp[2][lane] + s.qp[3][lane]);
      return u32x2{cvtpk(y[0], y[1]), cvtpk(y[2], y[3])};
    };
    const int ppos = c8 * 32 + (bit3 ? 16 : 0);
    auto post = [&](auto S_) __attribute__((always_inline)) {
      constexpr int S = S_.value;
      unsigned tag;
      {
        unsigned epoch = __builtin_amdgcn_raw_buffer_load_b32(rw, 0, WS_E, 16);
        if constexpr (S < 2) {
          u32x4 xr = ld_c(mk_rsrc(a.x + w * D, D * 2), lane * 16, 0);
          if constexpr (S == 0 && X_Q) proj_load(mk_rsrc(a.wq, D * HD * 2), HD * 2, (DH * h + 64 * b) * 2);
          SB();
          wg_barrier();
          PIN(epoch); PIN(xr);
          f32x2 v[4];
#pragma unroll
          for (int e = 0; e < 4; ++e) v[e] = f32x2{lo_f(xr[e]), hi_f(xr[e])};
          const f32x2 s2 = (v[0] + v[1]) + (v[2] + v[3]);
          const float mean = wave_sum(s2[0] + s2[1]) * (1.f / D);
          f32x2 q2 = {0, 0};
#pragma unroll
          for (int e = 0; e < 4; ++e) { const f32x2 dd = v[e] - mean; q2 = __builtin_elementwise_fma(dd, dd, q2); }
          const float var = rbf(wave_sum(lane < 56 ? q2[0] + q2[1] : 0.f) * (1.f / D));
          const float den = rbf(sqrtf(rbf(var + 1e-5f)));
          const float rinv = __builtin_amdgcn_rcpf(den);
          const float mb = rbf(mean);
          if (lane < 56) {
#pragma unroll
            for (int e = 0; e < 4; ++e) {
              const f32x2 dd = v[e] - mb;
              const unsigned tt = cvtpk(dd[0], dd[1]);
              s.xn[4 * lane + e][w] = cvtpk(lo_f(tt) * rinv, hi_f(tt) * rinv);
            }
          }
          tag = __builtin_amdgcn_readfirstlane(epoch) * 0x9E3779B1u + 0x7F4A7C15u;
          if constexpr (S == 0 && X_Q) {
            lds_signal(&s.cnt[CNT_X], lane);
            lds_wait<0>(&s.cnt[CNT_X], 4);
            if (w == 0) TS(10);
            proj_mac();
            if (lds_arrive(&s.cnt[CNT_Q], lane) == 3) {
              const u32x2 y = proj_sum();
              st_sc2(rw, (lane >> 4) * (NH * 1024) + ppos, WS_A + h * 1024 + b * 256, y[0], y[1], tag);
              TS(11);
            }
          }
        } else {
          wg_barrier();
          PIN(epoch);
          tag = __builtin_amdgcn_readfirstlane(epoch) * 0x9E3779B1u + 0x7F4A7C15u;
        }
        if (w == 1 && X_Q) {
          u32x4 v[1];
          poll<1, APS>(v, rw, lane * 16, WS_A + bh * 1024, 0, tag);
          *(u32x2*)&s.q[2 * lane] = u32x2{v[0][0], v[0][2]};
          TS(12);
        }
      }
      wg_barrier();
      constexpr int NL = S < 2 ? 1 : S, L0 = S < 3 ? S : 4;
      const int rc = S * 16 + h;
      const bool reducer = S < 2 && rc < 28;
      const bool ffn = X_FFN && bh < 56;
      if (S == 1 && ffn) {
        const int m = bh >= 28, cg = bh - 28 * m;
        proj_load(mk_rsrc(m ? a.wu : a.wg, D * F * 2), F * 2, cg * 128);
        SB();
        proj_mac();
        if (lds_arrive(&s.cnt[CNT_F], lane) == 3) {
          const u32x2 y = proj_sum();
          st_sc2(rw, (lane >> 4) * 7168 + ppos, WS_F + m * 28672 + cg * 256, y[0], y[1], tag);
          TS(14);
        }
      }
      const int dpart = bh / 7, dcg = bh - 7 * dpart;
      u32x4 da[4], db[4];
      if (S == 2 && ffn) {
        const rsrc_t rd = mk_rsrc(a.wd, F * D * 2);
        const int vo = 2 * r8 * (D * 2) + c8 * 16, so = (224 * dpart + 16 * w) * (D * 2) + dcg * 128;
#pragma unroll
        for (int m = 0; m < 4; ++m) { const int v_ = m < 3 || w < 2 ? vo : OOB; da[m] = ld_w(rd, v_, so + 64 * m * (D * 2)); db[m] = ld_w(rd, v_, so + (64 * m + 1) * (D * 2)); }
      }
      u32x4 wv[NL][8];
      {
        const rsrc_t ro = mk_rsrc(a.wo + (size_t)(h * DH + 64 * w) * D, 64 * D * 2);
        const int vrow = 2 * r8 * (D * 2) + c8 * 16;
#pragma unroll
        for (int j = 0; j < NL; ++j)
#pragma unroll
          for (int i0 = 0; i0 < 8; i0 += WOB) {
#pragma unroll
            for (int i = i0; i < i0 + WOB; ++i) wv[j][i] = ld_w(ro, X_WO ? vrow : OOB, (16 * (i >> 1) + (i & 1)) * (D * 2) + (L0 + j) * 128);
            SB();
#pragma unroll
            for (int p = i0 / 2; p < (i0 + WOB) / 2; ++p)
#pragma unroll
              for (int e = 0; e < 4; ++e) {
                unsigned lo = perm_lo(wv[j][2 * p][e], wv[j][2 * p + 1][e]), hi = perm_hi(wv[j][2 * p][e], wv[j][2 * p + 1][e]);
                PIN(lo); PIN(hi);
                wv[j][2 * p][e] = lo; wv[j][2 * p + 1][e] = hi;
              }
            SB();
          }
      }
      if (w == 1) TS(17);
      if (S == 2 && ffn) {
        u32x4 v[2];
        poll<2, 8>(v, rw, lane < 56 ? lane * 16 : OOB, WS_F + w * 7168 + dpart * 896, 28672, tag, false, lane >= 56);
        float hv[4];
#pragma unroll
        for (int e = 0; e < 4; ++e) {
          const float g = e & 1 ? hi_f(v[0][e & 2]) : lo_f(v[0][e & 2]), u = e & 1 ? hi_f(v[1][e & 2]) : lo_f(v[1][e & 2]);
          const float inner = 0.7978845608028654f * (g + 0.044715f * g * g * g);
          hv[e] = rbf(0.5f * g * (1.f + tanhf(inner))) * u;
        }
        if (lane < 56) { s.hT[2 * lane][w] = cvtpk(hv[0], hv[1]); s.hT[2 * lane + 1][w] = cvtpk(hv[2], hv[3]); }
        lds_signal(&s.cnt[CNT_F], lane);
        lds_wait<1>(&s.cnt[CNT_F], 4);
        if (w == 3) TS(15);
        float acc[4][8] = {};
#pragma unroll
        for (int m = 0; m < 4; ++m) {
          u32x4 x4 = *(const u32x4*)&s.hT[m < 3 || w < 2 ? 32 * m + 8 * w + r8 : 0][0];
          mac32(acc, da[m], db[m], x4);
        }
        float v4[4];
        red32(acc, bit3, v4);
        float* fp = &s.fp[w][(lane >> 4) * 64 + 8 * c8 + (bit3 ? 2 : 0)];
        *(f32x2*)fp = f32x2{v4[0], v4[1]}; *(f32x2*)(fp + 4) = f32x2{v4[2], v4[3]};
        if (lds_arrive(&s.cnt[CNT_F], lane) == 7) {
#pragma unroll
          for (int t2 = 0; t2 < 2; ++t2) {
            const int i = 128 * t2 + 2 * lane;
            const f32x2 y = (*(const f32x2*)&s.fp[0][i] + *(const f32x2*)&s.fp[1][i]) + (*(const f32x2*)&s.fp[2][i] + *(const f32x2*)&s.fp[3][i]);
            st_sc(rw, (lane >> 5) * (28 * 1024) + ((lane >> 3) & 3) * 1024 + c8 * 16, WS_D + (2 * t2 * 28 + 4 * dcg) * 1024 + dpart * 128, y[0], y[1], tag);
          }
          TS(16);
        }
      }
      float y0 = 0.f, y1 = 0.f;
      unsigned xw = 0;
      if (S < 2 && w == 3) {
        if (reducer) {
          xw = __builtin_amdgcn_raw_buffer_load_b32(mk_rsrc(a.x, 4 * D * 2), c8 * 4, (b * D + 16 * rc) * 2, 0);
          u32x4 v[1];
          if (X_FFN) {
            lds_wait<16>(&s.cnt[CNT_K], 4);
            poll<1, 16>(v, rw, lane * 16, WS_D + (b * 28 + rc) * 1024, 0, tag);
            y0 = __uint_as_float(v[0][0]); y1 = __uint_as_float(v[0][2]);
          }
          y0 = rbf(hi3_sum(y0)); y1 = rbf(hi3_sum(y1));
          if (b == 0 && rc == 0) {
            lds_wait<16>(&s.cnt[CNT_K], 4);
            u32x4 vm[4];
            poll<4, 16>(vm, rw, lane * 16, WS_M, 1024, tag);
            if (EARLYE) __builtin_amdgcn_raw_buffer_store_b32(tag, rw, lane == 0 ? 0 : OOB, WS_E, 16);
          }
          TS(18);
        }
      }
      if (w == 2) {
        lds_wait<4>(&s.cnt[CNT_K], 4);
        const float mw = s.ml[lane & 3][0], lw = s.ml[lane & 3][1];
        float m = fmaxf(mw, dpp<XOR1>(mw)); m = fmaxf(m, dpp<XOR2>(m));
        float l = lw * __expf(mw - m); l += dpp<XOR1>(l); l += dpp<XOR2>(l);
        st_sc(rw, lane == 0 ? 0 : OOB, WS_M + bh * 64 + S * 16, m, l, tag);
        lds_wait<16>(&s.cnt[CNT_K], 8);
        u32x4 v[1];
        poll<1, 8>(v, rw, (lane & 3) * 16, WS_M + bh * 64, 0, tag);
        const float ms = __uint_as_float(v[0][0]);
        float M = fmaxf(ms, dpp<XOR1>(ms)); M = fmaxf(M, dpp<XOR2>(M));
        float L = __uint_as_float(v[0][2]) * __expf(ms - M); L += dpp<XOR1>(L); L += dpp<XOR2>(L);
        if (lane < 4) s.sf[lane] = __expf(mw - M) / L;
        lds_signal(&s.cnt[CNT_V], lane);
        TS(19);
      }
      u32x4 fl[4];
      lds_wait<1>(&s.cnt[CNT_V], 5);
      {
        const int dim = 64 * w + 2 * (lane & 31);
        const f32x4 f = *(const f32x4*)s.sf;
        const f32x2 o = (*(const f32x2*)&s.so[0][dim] * f[0] + *(const f32x2*)&s.so[1][dim] * f[1]) + (*(const f32x2*)&s.so[2][dim] * f[2] + *(const f32x2*)&s.so[3][dim] * f[3]);
        st_sc(rw, lane < 32 ? lane * 16 : OOB, WS_B + id * 2048 + w * 512, o[0], o[1], tag);
        u32x4 v[2];
        poll2<PINC>(v, fl, rw, (c8 >> 2) * 2048 + (8 * (c8 & 3) + r8) * 16, WS_B + bh * 8192 + w * 512, 4096, tag);
        if (w == 2) TS(20);
        float oa = __uint_as_float(v[0][0]) + __uint_as_float(v[1][0]), ob = __uint_as_float(v[0][2]) + __uint_as_float(v[1][2]);
        oa += dpp<0x104>(oa); ob += dpp<0x104>(ob);
        const int opk = cvtpk(oa, ob);
        float acc[NL][8] = {};
        static_for<4>([&](auto P) __attribute__((always_inline)) {
          constexpr int p = P.value;
          int op = __builtin_amdgcn_update_dpp(0, opk, 0x150 + p, 0xf, 0x3, false);
          op = __builtin_amdgcn_update_dpp(op, opk, 0x158 + p, 0xf, 0xc, false);
#pragma unroll
          for (int j = 0; j < NL; ++j)
#pragma unroll
            for (int e = 0; e < 4; ++e) { acc[j][2 * e] = dot2(wv[j][2 * p][e], op, acc[j][2 * e]); acc[j][2 * e + 1] = dot2(wv[j][2 * p + 1][e], op, acc[j][2 * e + 1]); }
        });
#pragma unroll
        for (int j = 0; j < NL; ++j) {
          float u[4];
#pragma unroll
          for (int e = 0; e < 4; ++e) u[e] = swap32_add(acc[j][e], acc[j][e + 4]);
          s.sy[w][64 * j + 8 * c8 + r8] = comb<XOR8>(swap16_add(u[0], u[2]), swap16_add(u[1], u[3]), bit3);
        }
      }
      FENCE();
      asm volatile("s_waitcnt lgkmcnt(0)" ::: "memory");
      int arrived = 0;
      if (lane == 0) arrived = __hip_atomic_fetch_add(&s.cnt[CNT_H], 1, __ATOMIC_RELAXED, __HIP_MEMORY_SCOPE_WORKGROUP);
      arrived = __builtin_amdgcn_readfirstlane(arrived);
      FENCE();
      if (arrived == 3) {
#pragma unroll
        for (int t2 = 0; t2 < (NL + 1) / 2; ++t2) {
          const bool on = lane < 32 * (NL - 2 * t2);
          const int col = 128 * t2 + 2 * (on ? lane : 0);
          const f32x2 y = (*(const f32x2*)&s.sy[0][col] + *(const f32x2*)&s.sy[1][col]) + (*(const f32x2*)&s.sy[2][col] + *(const f32x2*)&s.sy[3][col]);
          st_sc(rw, on ? r8 * 2048 + c8 * 16 : OOB, WS_C + (b * 28 + 4 * L0 + 8 * t2) * 2048 + h * 128, y[0], y[1], tag);
        }
        TS(21);
      }
      if (PINC == 2) { PIN(fl[0]); PIN(fl[1]); PIN(fl[2]); PIN(fl[3]); }
      if (S < 2 && w == 3 && reducer) {
        u32x4 v[2];
        poll2<PINC>(v, fl, rw, lane * 16, WS_C + (b * 28 + rc) * 2048, 1024, tag);
        const float a0 = rbf(hi3_sum(__uint_as_float(v[0][0]) + __uint_as_float(v[1][0]))), a1 = rbf(hi3_sum(__uint_as_float(v[0][2]) + __uint_as_float(v[1][2])));
        const float r0 = rbf(lo_f(xw) + a0) + y0, r1 = rbf(hi_f(xw) + a1) + y1;
        __builtin_amdgcn_raw_buffer_store_b32(cvtpk(r0, r1), mk_rsrc(a.out, 4 * D * 2), lane < 8 ? lane * 4 : OOB, (b * D + 16 * rc) * 2, OUTAUX);
        if (!EARLYE && b == 0 && rc == 0) __builtin_amdgcn_raw_buffer_store_b32(tag, rw, lane == 0 ? 0 : OOB, WS_E, 16);
        TS(22);
        if (PINC == 2) { PIN(fl[0]); PIN(fl[1]); PIN(fl[2]); PIN(fl[3]); }
      }
    };
    if (sidx == 0) post(IC<0>{}); else if (sidx == 1) post(IC<1>{}); else if (sidx == 2) post(IC<2>{}); else post(IC<3>{});
  }
}
}

extern "C" void kernel_launch(void* const* d_in, const int* in_sizes, int n_in, void* d_out, int out_size, void* d_ws,
                              size_t ws_size, hipStream_t stream) {
  Args a;
  a.x = (const unsigned short*)d_in[0]; a.K = (const unsigned short*)d_in[1]; a.V = (const unsigned short*)d_in[2];
  a.wq = (const unsigned short*)d_in[3]; a.wo = (const unsigned short*)d_in[4];
  a.wg = (const unsigned short*)d_in[6]; a.wu = (const unsigned short*)d_in[7]; a.wd = (const unsigned short*)d_in[8];
  a.out = (unsigned*)d_out; a.ws = (char*)d_ws;
  k_main<<<256, 512, 0, stream>>>(a);
}
```

```cpp
#include <hip/hip_runtime.h>
#include <hip/hip_bf16.h>
#include <stdint.h>

namespace {
constexpr int W0 = 40;
constexpr int WV = 16;
constexpr int CDIV = 2;
constexpr int WOB = 4;
constexpr int SPIN_MAX = 100000;
constexpr int NT = 2;
constexpr int SC1 = 16;
constexpr int D = 448, NH = 16, DH = 256, F = 1792, HD = NH * DH;
constexpr unsigned WS_E = 0;
constexpr unsigned WS_A = 4096;
constexpr unsigned WS_M = WS_A + 65536;
constexpr unsigned WS_F = WS_M + 4096;
constexpr unsigned WS_D = WS_F + 57344;
constexpr unsigned WS_B = WS_D + 114688;
constexpr unsigned WS_C = WS_B + 524288;
constexpr unsigned WS_END = WS_C + 229376;
constexpr int OOB = 0x40000000;

typedef unsigned u32x4 __attribute__((ext_vector_type(4)));
typedef unsigned u32x2 __attribute__((ext_vector_type(2)));
typedef float f32x4 __attribute__((ext_vector_type(4)));
typedef float f32x2 __attribute__((ext_vector_type(2)));
typedef __bf16 bf16x2 __attribute__((ext_vector_type(2)));
typedef __amdgpu_buffer_rsrc_t rsrc_t;

__device__ __forceinline__ rsrc_t mk_rsrc(const void* p, unsigned bytes) {
  return __builtin_amdgcn_make_buffer_rsrc((void*)p, 0, bytes, 0x00020000);
}
__device__ __forceinline__ u32x4 ld_nt(rsrc_t r, int voff, int soff) { return __builtin_amdgcn_raw_buffer_load_b128(r, voff, soff, NT); }
__device__ __forceinline__ u32x4 ld_c(rsrc_t r, int voff, int soff) { return __builtin_amdgcn_raw_buffer_load_b128(r, voff, soff, 0); }
__device__ __forceinline__ u32x4 ld_sc(rsrc_t r, int voff, int soff) { return __builtin_amdgcn_raw_buffer_load_b128(r, voff, soff, SC1); }
__device__ __forceinline__ void st_sc(rsrc_t r, int voff, int soff, float a, float b, unsigned tag) {
  __builtin_amdgcn_raw_buffer_store_b128(u32x4{__float_as_uint(a), tag, __float_as_uint(b), tag}, r, voff, soff, SC1);
}
__device__ __forceinline__ void st_sc2(rsrc_t r, int voff, int soff, unsigned a, unsigned b, unsigned tag) {
  __builtin_amdgcn_raw_buffer_store_b128(u32x4{a, tag, b, tag}, r, voff, soff, SC1);
}
__device__ __forceinline__ float lo_f(unsigned w) { return __uint_as_float(w << 16); }
__device__ __forceinline__ float hi_f(unsigned w) { return __uint_as_float(w & 0xffff0000u); }
__device__ __forceinline__ unsigned cvtpk(float lo, float hi) {
  const bf16x2 r = {(__bf16)lo, (__bf16)hi}; return __builtin_bit_cast(unsigned, r);
}
__device__ __forceinline__ float rbf(float x) { return lo_f(cvtpk(x, x)); }
__device__ __forceinline__ float dot2(unsigned a, unsigned b, float acc) {
  return __builtin_amdgcn_fdot2_f32_bf16(__builtin_bit_cast(bf16x2, a), __builtin_bit_cast(bf16x2, b), acc, false);
}
__device__ __forceinline__ unsigned perm_lo(unsigned a, unsigned b) { return __builtin_amdgcn_perm(b, a, 0x05040100u); }
__device__ __forceinline__ unsigned perm_hi(unsigned a, unsigned b) { return __builtin_amdgcn_perm(b, a, 0x07060302u); }
template <int CTRL> __device__ __forceinline__ float dpp(float x) {
  return __builtin_bit_cast(float, __builtin_amdgcn_mov_dpp(__builtin_bit_cast(int, x), CTRL, 0xf, 0xf, true));
}
constexpr int XOR1 = 0xB1, XOR2 = 0x4E, XOR7 = 0x141, XOR8 = 0x128;
__device__ __forceinline__ float swap16_add(float a, float b) {
  auto s = __builtin_amdgcn_permlane16_swap(__float_as_uint(a), __float_as_uint(b), false, false);
  return __uint_as_float(s[0]) + __uint_as_float(s[1]);
}
__device__ __forceinline__ float swap32_add(float a, float b) {
  auto s = __builtin_amdgcn_permlane32_swap(__float_as_uint(a), __float_as_uint(b), false, false);
  return __uint_as_float(s[0]) + __uint_as_float(s[1]);
}
__device__ __forceinline__ float swap16_max(float v) {
  auto s = __builtin_amdgcn_permlane16_swap(__float_as_uint(v), __float_as_uint(v), false, false);
  return fmaxf(__uint_as_float(s[0]), __uint_as_float(s[1]));
}
__device__ __forceinline__ float swap32_max(float v) {
  auto s = __builtin_amdgcn_permlane32_swap(__float_as_uint(v), __float_as_uint(v), false, false);
  return fmaxf(__uint_as_float(s[0]), __uint_as_float(s[1]));
}
template <int CTRL> __device__ __forceinline__ float comb(float a, float b, bool sel) {
  const float t1 = a + dpp<CTRL>(a), t2 = b + dpp<CTRL>(b);
  return sel ? t2 : t1;
}
__device__ __forceinline__ float row_sum(float v) { v += dpp<XOR1>(v); v += dpp<XOR2>(v); v += dpp<XOR7>(v); return v + dpp<XOR8>(v); }
__device__ __forceinline__ float hi3_sum(float v) { v += dpp<XOR8>(v); v = swap16_add(v, v); return swap32_add(v, v); }
__device__ __forceinline__ float hi3_max(float v) { v = fmaxf(v, dpp<XOR8>(v)); return swap32_max(swap16_max(v)); }
__device__ __forceinline__ float wave_sum(float v) { v = row_sum(v); v = swap16_add(v, v); return swap32_add(v, v); }
__device__ __forceinline__ float wave_max(float v) {
  v = fmaxf(v, dpp<XOR1>(v)); v = fmaxf(v, dpp<XOR2>(v)); v = fmaxf(v, dpp<XOR7>(v)); return hi3_max(v);
}
__device__ __forceinline__ void wg_barrier() { asm volatile("s_waitcnt lgkmcnt(0)\n\ts_barrier" ::: "memory"); }
#define SB() __builtin_amdgcn_sched_barrier(0)
#define PIN(x) asm volatile("" : "+v"(x))
#define FENCE() asm volatile("" ::: "memory")

__device__ __forceinline__ void red32(const float (&acc)[4][8], bool bit3, float (&v)[4]) {
  float u[8];
#pragma unroll
  for (int e = 0; e < 8; ++e) u[e] = swap16_add(swap32_add(acc[0][e], acc[2][e]), swap32_add(acc[1][e], acc[3][e]));
  v[0] = comb<XOR8>(u[0], u[2], bit3); v[1] = comb<XOR8>(u[1], u[3], bit3);
  v[2] = comb<XOR8>(u[4], u[6], bit3); v[3] = comb<XOR8>(u[5], u[7], bit3);
}
__device__ __forceinline__ f32x4 red32c(const float (&acc)[4][8], bool bit3) {
  float u[8];
#pragma unroll
  for (int e = 0; e < 8; ++e) u[e] = swap16_add(swap32_add(acc[0][e], acc[2][e]), swap32_add(acc[1][e], acc[3][e]));
  return f32x4{comb<XOR8>(u[0], u[4], bit3), comb<XOR8>(u[1], u[5], bit3), comb<XOR8>(u[2], u[6], bit3), comb<XOR8>(u[3], u[7], bit3)};
}
__device__ __forceinline__ void mac32(float (&acc)[4][8], u32x4 A, u32x4 B, u32x4 x4) {
#pragma unroll
  for (int e = 0; e < 4; ++e) {
    const unsigned pl = perm_lo(A[e], B[e]), ph = perm_hi(A[e], B[e]);
#pragma unroll
    for (int bb = 0; bb < 4; ++bb) { acc[bb][2 * e] = dot2(pl, x4[bb], acc[bb][2 * e]); acc[bb][2 * e + 1] = dot2(ph, x4[bb], acc[bb][2 * e + 1]); }
  }
}
template <int N, int SLEEP> __device__ __forceinline__ void poll(u32x4 (&v)[N], rsrc_t r, int voff, int soff, int stride, unsigned tag, bool skip_all = false) {
#pragma unroll 1
  for (int it = 0; it < SPIN_MAX; ++it) {
    FENCE();
#pragma unroll
    for (int i = 0; i < N; ++i) v[i] = ld_sc(r, voff, soff + i * stride);
    bool ok = true;
#pragma unroll
    for (int i = 0; i < N; ++i) { const bool g = v[i][1] == tag && v[i][3] == tag; ok = ok && (g || skip_all); }
    if (__builtin_amdgcn_ballot_w64(!ok) == 0) break;
    if (SLEEP) __builtin_amdgcn_s_sleep(SLEEP);
  }
}
__device__ __forceinline__ void poll2(u32x4 (&v)[2], rsrc_t r, int voff, int soff, int stride, unsigned tag) {
  int o0 = voff, o1 = voff;
  unsigned a0 = 0, a1 = 0, b0 = 0, b1 = 0;
#pragma unroll 1
  for (int it = 0; it < SPIN_MAX; ++it) {
    FENCE();
    const u32x4 x = ld_sc(r, o0, soff), y = ld_sc(r, o1, soff + stride);
    const bool gx = x[1] == tag && x[3] == tag, gy = y[1] == tag && y[3] == tag;
    a0 = gx ? x[0] : a0; a1 = gx ? x[2] : a1; o0 = gx ? OOB : o0;
    b0 = gy ? y[0] : b0; b1 = gy ? y[2] : b1; o1 = gy ? OOB : o1;
    if (__builtin_amdgcn_ballot_w64((o0 & o1) != OOB) == 0) break;
  }
  v[0][0] = a0; v[0][2] = a1; v[1][0] = b0; v[1][2] = b1;
}
template <int SLEEP> __device__ __forceinline__ void lds_wait(int* p, int n) {
#pragma unroll 1
  for (int it = 0; it < SPIN_MAX * 64; ++it) {
    FENCE();
    if (__builtin_amdgcn_readfirstlane(__hip_atomic_load(p, __ATOMIC_RELAXED, __HIP_MEMORY_SCOPE_WORKGROUP)) >= n) break;
    if (SLEEP) __builtin_amdgcn_s_sleep(SLEEP);
  }
  FENCE();
}
__device__ __forceinline__ int lds_arrive(int* p, int lane) {
  FENCE();
  int n = 0;
  if (lane == 0) n = __hip_atomic_fetch_add(p, 1, __ATOMIC_RELAXED, __HIP_MEMORY_SCOPE_WORKGROUP);
  n = __builtin_amdgcn_readfirstlane(n);
  FENCE();
  return n;
}
__device__ __forceinline__ void lds_signal(int* p, int lane) {
  FENCE();
  if (lane == 0) __hip_atomic_fetch_add(p, 1, __ATOMIC_RELAXED, __HIP_MEMORY_SCOPE_WORKGROUP);
  FENCE();
}

struct Args {
  const unsigned short* x; const unsigned short* K; const unsigned short* V; const unsigned short* wq; const unsigned short* wo;
  const unsigned short* wg; const unsigned short* wu; const unsigned short* wd;
  unsigned* out; char* ws;
};

enum { CNT_K, CNT_V, CNT_H, CNT_F, CNT_X, CNT_Q };
struct __attribute__((aligned(16))) Lds {
  float sp[4][2][128];
  float so[4][DH];
  float sy[4][192];
  float sf[4];
  unsigned xn[224][4];
  unsigned hT[112][4];
  float fp[4][256];
  f32x4 qp[4][64];
  unsigned q[128];
  float ml[4][2];
  int cnt[8];
};

template <int V> struct IC { static constexpr int value = V; };
template <int N, int I = 0, class Fn> __device__ __forceinline__ void static_for(Fn&& f) {
  if constexpr (I < N) { f(IC<I>{}); static_for<N, I + 1>(f); }
}
constexpr int issued(int j) {
  return j < 0 ? W0 : (W0 + (j + 1) / CDIV > j + 1 + WV ? W0 + (j + 1) / CDIV : j + 1 + WV);
}

__global__ void __launch_bounds__(512, 2) k_main(Args a) {
  __shared__ Lds s;
  const int t = threadIdx.x, lane = t & 63, w = __builtin_amdgcn_readfirstlane(t >> 6);
  const int blk = blockIdx.x, xc = blk & 7, r = blk >> 3;
  const int h = 2 * xc + (r & 1), b = r >> 3, sidx = (r >> 1) & 3;
  const int bh = b * NH + h, id = bh * 4 + sidx;
  const rsrc_t rw = mk_rsrc(a.ws, WS_END);
  if (t < 8) s.cnt[t] = 0;

  if (w >= 4) {
    const int ws_ = w - 4;
    const int c = lane & 31, hf = lane >> 5;
    const size_t base = (size_t)id * (1024 * DH) + (size_t)ws_ * 512;
    const unsigned span = 512 * 1024 - ws_ * 1024;
    const rsrc_t rk = mk_rsrc(a.K + base, span), rv = mk_rsrc(a.V + base, span);
    int off = ((blk >> 1) & 127) * 4096;
    const int vo = lane * 16;
    wg_barrier();
    u32x4 kb[128], vb[WV];
    static_for<W0>([&](auto J) __attribute__((always_inline)) {
      constexpr int j = J.value;
      kb[j] = ld_nt(rk, vo, off); off = (off + 4096) & 0x7ffff;
      SB();
    });
    wg_barrier();
    const u32x4 qf = *(const u32x4*)&s.q[4 * c];
    const bool c8 = c & 8, c4 = c & 4, c2 = c & 2, c1 = c & 1;
    float sc[4];
    {
      float p0, p1, p2, p3, p4;
      static_for<128>([&](auto J) __attribute__((always_inline)) {
        constexpr int j = J.value;
        float d = dot2(kb[j][0], qf[0], 0.f); d = dot2(kb[j][1], qf[1], d); d = dot2(kb[j][2], qf[2], d); d = dot2(kb[j][3], qf[3], d);
        PIN(d); SB();
        if constexpr (issued(j) > issued(j - 1)) {
          constexpr int n = issued(j - 1);
          if constexpr (n < 128) kb[n] = ld_nt(rk, vo, off); else vb[n - 128] = ld_nt(rv, vo, off);
          off = (off + 4096) & 0x7ffff;
        }
        if constexpr (!(j & 1)) p0 = d; else { d = swap16_add(p0, d);
          if constexpr (!(j & 2)) p1 = d; else { d = comb<XOR8>(p1, d, c8);
            if constexpr (!(j & 4)) p2 = d; else { d = comb<XOR7>(p2, d, c4);
              if constexpr (!(j & 8)) p3 = d; else { d = comb<XOR2>(p3, d, c2);
                if constexpr (!(j & 16)) p4 = d; else { d = comb<XOR1>(p4, d, c1); sc[j >> 5] = rbf(d) * 0.0625f; } } } } }
        SB();
      });
    }
    const float m = wave_max(fmaxf(fmaxf(sc[0], sc[1]), fmaxf(sc[2], sc[3])));
    const float e0 = __expf(sc[0] - m), e1 = __expf(sc[1] - m), e2 = __expf(sc[2] - m), e3 = __expf(sc[3] - m);
    const int br = __builtin_bitreverse32((unsigned)c) >> 27;
    float* sp = &s.sp[ws_][hf][0];
    sp[br] = e0; sp[32 + br] = e1; sp[64 + br] = e2; sp[96 + br] = e3;
    const float l = wave_sum((e0 + e1) + (e2 + e3));
    if (lane == 0) { s.ml[ws_][0] = m; s.ml[ws_][1] = l; }
    lds_signal(&s.cnt[CNT_K], lane);
    asm volatile("s_waitcnt lgkmcnt(0)" ::: "memory");
    f32x2 acc[4] = {{0, 0}, {0, 0}, {0, 0}, {0, 0}};
    const f32x4* sp4 = (const f32x4*)sp;
    f32x4 pc, pn = sp4[0];
#define VSTEP(j_) { if (!((j_) & 3)) { pc = pn; pn = sp4[(g * (WV / 4) + (j_) / 4 + 1) & 31]; } \
        const u32x4 v = vb[j_]; const float p = pc[(j_) & 3]; const f32x2 pp = {p, p}; \
        _Pragma("unroll") for (int k = 0; k < 4; ++k) { const f32x2 vv = {lo_f(v[k]), hi_f(v[k])}; acc[k] = __builtin_elementwise_fma(vv, pp, acc[k]); PIN(acc[k]); } }
#pragma unroll 1
    for (int g = 0; g < 128 / WV - 1; ++g) {
#pragma unroll
      for (int j = 0; j < WV; ++j) {
        VSTEP(j); SB();
        vb[j] = ld_nt(rv, vo, off); off = (off + 4096) & 0x7ffff; SB();
      }
      if (g == 64 / WV - 1) lds_signal(&s.cnt[CNT_K], lane);
    }
    {
      const int g = 128 / WV - 1;
#pragma unroll
      for (int j = 0; j < WV; ++j) { VSTEP(j); SB(); }
    }
#undef VSTEP
    {
      const float o0 = swap32_add(acc[0][0], acc[1][0]), o1 = swap32_add(acc[0][1], acc[1][1]);
      const float o2 = swap32_add(acc[2][0], acc[3][0]), o3 = swap32_add(acc[2][1], acc[3][1]);
      float* so = &s.so[ws_][8 * c + 2 * hf];
      *(f32x2*)so = f32x2{o0, o1}; *(f32x2*)(so + 4) = f32x2{o2, o3};
    }
    lds_signal(&s.cnt[CNT_V], lane);
  } else {
    __builtin_amdgcn_s_setprio(3);
    const int r8 = lane >> 3, c8 = lane & 7;
    const bool bit3 = lane & 8;
    u32x4 pa[7], pb[7];
    auto proj_load = [&](rsrc_t r, int stride, int col) __attribute__((always_inline)) {
      const int vo = 2 * r8 * stride + c8 * 16, so = 112 * w * stride + col;
#pragma unroll
      for (int k = 0; k < 7; ++k) { pa[k] = ld_c(r, vo, so + 16 * k * stride); pb[k] = ld_c(r, vo, so + (16 * k + 1) * stride); }
    };
    auto proj_mac = [&]() __attribute__((always_inline)) {
      float acc[4][8] = {};
#pragma unroll
      for (int k = 0; k < 7; ++k) mac32(acc, pa[k], pb[k], *(const u32x4*)&s.xn[56 * w + 8 * k + r8][0]);
      s.qp[w][lane] = red32c(acc, bit3);
    };
    auto proj_sum = [&]() __attribute__((always_inline)) {
      const f32x4 y = (s.qp[0][lane] + s.qp[1][lane]) + (s.qp[2][lane] + s.qp[3][lane]);
      return u32x2{cvtpk(y[0], y[1]), cvtpk(y[2], y[3])};
    };
    const int ppos = c8 * 32 + (bit3 ? 16 : 0);
    auto post = [&](auto S_) __attribute__((always_inline)) {
      constexpr int S = S_.value;
      unsigned tag, pre0 = 0, pre1 = 0;
      {
        unsigned epoch = __builtin_amdgcn_raw_buffer_load_b32(rw, 0, WS_E, SC1);
        if constexpr (S < 2) {
          u32x4 xr = ld_c(mk_rsrc(a.x + w * D, D * 2), lane * 16, 0);
          if constexpr (S == 0) proj_load(mk_rsrc(a.wq, D * HD * 2), HD * 2, (DH * h + 64 * b) * 2);
          SB();
          wg_barrier();
          SB();
          PIN(epoch); PIN(xr);
          f32x2 v[4];
#pragma unroll
          for (int e = 0; e < 4; ++e) v[e] = f32x2{lo_f(xr[e]), hi_f(xr[e])};
          const f32x2 s2 = (v[0] + v[1]) + (v[2] + v[3]);
          const float mean = wave_sum(s2[0] + s2[1]) * (1.f / D);
          f32x2 q2 = {0, 0};
#pragma unroll
          for (int e = 0; e < 4; ++e) { const f32x2 dd = v[e] - mean; q2 = __builtin_elementwise_fma(dd, dd, q2); }
          const float var = rbf(wave_sum(lane < 56 ? q2[0] + q2[1] : 0.f) * (1.f / D));
          const float den = rbf(sqrtf(rbf(var + 1e-5f)));
          const float rinv = __builtin_amdgcn_rcpf(den);
          const float mb = rbf(mean);
          if (lane < 56) {
#pragma unroll
            for (int e = 0; e < 4; ++e) {
              const f32x2 dd = v[e] - mb;
              const unsigned tt = cvtpk(dd[0], dd[1]);
              s.xn[4 * lane + e][w] = cvtpk(lo_f(tt) * rinv, hi_f(tt) * rinv);
            }
          }
          tag = __builtin_amdgcn_readfirstlane(epoch) * 0x9E3779B1u + 0x7F4A7C15u;
          if constexpr (S == 0) {
            lds_signal(&s.cnt[CNT_X], lane);
            lds_wait<0>(&s.cnt[CNT_X], 4);
            proj_mac();
            if (lds_arrive(&s.cnt[CNT_Q], lane) == 3) {
              const u32x2 y = proj_sum();
              st_sc2(rw, (lane >> 4) * (NH * 1024) + ppos, WS_A + h * 1024 + b * 256, y[0], y[1], tag);
            }
          }
        } else {
          SB();
          wg_barrier();
          SB();
          PIN(epoch);
          tag = __builtin_amdgcn_readfirstlane(epoch) * 0x9E3779B1u + 0x7F4A7C15u;
        }
        if (w == 3) {
          pre0 = __builtin_amdgcn_raw_buffer_load_b32(mk_rsrc(a.V + (size_t)id * (1024 * DH), 1024 * DH * 2), lane < 8 ? lane * 65536 : OOB, 0, 0);
          pre1 = __builtin_amdgcn_raw_buffer_load_b32(mk_rsrc(a.out, 4 * D * 2), lane < 1 ? 0 : OOB, 0, 0);
        }
        if (w == 1) {
          u32x4 v[1];
          poll<1, 0>(v, rw, lane * 16, WS_A + bh * 1024, 0, tag);
          *(u32x2*)&s.q[2 * lane] = u32x2{v[0][0], v[0][2]};
        }
      }
      wg_barrier();
      PIN(pre0); PIN(pre1);
      constexpr int NL = S < 2 ? 1 : S, L0 = S < 3 ? S : 4;
      const int rc = S * 16 + h;
      const bool reducer = S < 2 && rc < 28;
      const bool ffn = bh < 56;
      if (S == 1 && ffn) {
        const int m = bh >= 28, cg = bh - 28 * m;
        proj_load(mk_rsrc(m ? a.wu : a.wg, D * F * 2), F * 2, cg * 128);
        SB();
        proj_mac();
        if (lds_arrive(&s.cnt[CNT_F], lane) == 3) {
          const u32x2 y = proj_sum();
          st_sc2(rw, (lane >> 4) * 7168 + ppos, WS_F + m * 28672 + cg * 256, y[0], y[1], tag);
        }
      }
      const int dpart = bh / 7, dcg = bh - 7 * dpart;
      u32x4 da[4], db[4];
      if (S == 2 && ffn) {
        const rsrc_t rd = mk_rsrc(a.wd, F * D * 2);
        const int vo = 2 * r8 * (D * 2) + c8 * 16, so = (224 * dpart + 16 * w) * (D * 2) + dcg * 128;
#pragma unroll
        for (int m = 0; m < 4; ++m) { const int v_ = m < 3 || w < 2 ? vo : OOB; da[m] = ld_c(rd, v_, so + 64 * m * (D * 2)); db[m] = ld_c(rd, v_, so + (64 * m + 1) * (D * 2)); }
      }
      u32x4 wv[NL][8];
      {
        const rsrc_t ro = mk_rsrc(a.wo + (size_t)(h * DH + 64 * w) * D, 64 * D * 2);
        const int vrow = 2 * r8 * (D * 2) + c8 * 16;
#pragma unroll
        for (int j = 0; j < NL; ++j)
#pragma unroll
          for (int i0 = 0; i0 < 8; i0 += WOB) {
#pragma unroll
            for (int i = i0; i < i0 + WOB; ++i) wv[j][i] = ld_c(ro, vrow, (16 * (i >> 1) + (i & 1)) * (D * 2) + (L0 + j) * 128);
            SB();
#pragma unroll
            for (int p = i0 / 2; p < (i0 + WOB) / 2; ++p)
#pragma unroll
              for (int e = 0; e < 4; ++e) {
                unsigned lo = perm_lo(wv[j][2 * p][e], wv[j][2 * p + 1][e]), hi = perm_hi(wv[j][2 * p][e], wv[j][2 * p + 1][e]);
                PIN(lo); PIN(hi);
                wv[j][2 * p][e] = lo; wv[j][2 * p + 1][e] = hi;
              }
            SB();
          }
      }
      if (S == 2 && ffn) {
        u32x4 v[2];
        poll<2, 8>(v, rw, lane < 56 ? lane * 16 : OOB, WS_F + w * 7168 + dpart * 896, 28672, tag, lane >= 56);
        float hv[4];
#pragma unroll
        for (int e = 0; e < 4; ++e) {
          const float g = e & 1 ? hi_f(v[0][e & 2]) : lo_f(v[0][e & 2]), u = e & 1 ? hi_f(v[1][e & 2]) : lo_f(v[1][e & 2]);
          const float inner = 0.7978845608028654f * (g + 0.044715f * g * g * g);
          hv[e] = rbf(0.5f * g * (1.f + tanhf(inner))) * u;
        }
        if (lane < 56) { s.hT[2 * lane][w] = cvtpk(hv[0], hv[1]); s.hT[2 * lane + 1][w] = cvtpk(hv[2], hv[3]); }
        lds_signal(&s.cnt[CNT_F], lane);
        lds_wait<1>(&s.cnt[CNT_F], 4);
        float acc[4][8] = {};
#pragma unroll
        for (int m = 0; m < 4; ++m) {
          u32x4 x4 = *(const u32x4*)&s.hT[m < 3 || w < 2 ? 32 * m + 8 * w + r8 : 0][0];
          mac32(acc, da[m], db[m], x4);
        }
        float v4[4];
        red32(acc, bit3, v4);
        float* fp = &s.fp[w][(lane >> 4) * 64 + 8 * c8 + (bit3 ? 2 : 0)];
        *(f32x2*)fp = f32x2{v4[0], v4[1]}; *(f32x2*)(fp + 4) = f32x2{v4[2], v4[3]};
        if (lds_arrive(&s.cnt[CNT_F], lane) == 7) {
#pragma unroll
          for (int t2 = 0; t2 < 2; ++t2) {
            const int i = 128 * t2 + 2 * lane;
            const f32x2 y = (*(const f32x2*)&s.fp[0][i] + *(const f32x2*)&s.fp[1][i]) + (*(const f32x2*)&s.fp[2][i] + *(const f32x2*)&s.fp[3][i]);
            st_sc(rw, (lane >> 5) * (28 * 1024) + ((lane >> 3) & 3) * 1024 + c8 * 16, WS_D + (2 * t2 * 28 + 4 * dcg) * 1024 + dpart * 128, y[0], y[1], tag);
          }
        }
      }
      float y0 = 0.f, y1 = 0.f;
      unsigned xw = 0;
      if (S < 2 && w == 3) {
        if (reducer) {
          xw = __builtin_amdgcn_raw_buffer_load_b32(mk_rsrc(a.x, 4 * D * 2), c8 * 4, (b * D + 16 * rc) * 2, 0);
          u32x4 v[1];
          lds_wait<16>(&s.cnt[CNT_K], 4);
          poll<1, 16>(v, rw, lane * 16, WS_D + (b * 28 + rc) * 1024, 0, tag);
          y0 = __uint_as_float(v[0][0]); y1 = __uint_as_float(v[0][2]);
          y0 = rbf(hi3_sum(y0)); y1 = rbf(hi3_sum(y1));
          if (b == 0 && rc == 0) {
            u32x4 vm[4];
            poll<4, 16>(vm, rw, lane * 16, WS_M, 1024, tag);
            __builtin_amdgcn_raw_buffer_store_b32(tag, rw, lane == 0 ? 0 : OOB, WS_E, SC1);
          }
        }
      }
      if (w == 2) {
        lds_wait<4>(&s.cnt[CNT_K], 4);
        const float mw = s.ml[lane & 3][0], lw = s.ml[lane & 3][1];
        float m = fmaxf(mw, dpp<XOR1>(mw)); m = fmaxf(m, dpp<XOR2>(m));
        float l = lw * __expf(mw - m); l += dpp<XOR1>(l); l += dpp<XOR2>(l);
        st_sc(rw, lane == 0 ? 0 : OOB, WS_M + bh * 64 + S * 16, m, l, tag);
        lds_wait<16>(&s.cnt[CNT_K], 8);
        u32x4 v[1];
        poll<1, 8>(v, rw, (lane & 3) * 16, WS_M + bh * 64, 0, tag);
        const float ms = __uint_as_float(v[0][0]);
        float M = fmaxf(ms, dpp<XOR1>(ms)); M = fmaxf(M, dpp<XOR2>(M));
        float L = __uint_as_float(v[0][2]) * __expf(ms - M); L += dpp<XOR1>(L); L += dpp<XOR2>(L);
        if (lane < 4) s.sf[lane] = __expf(mw - M) / L;
        lds_signal(&s.cnt[CNT_V], lane);
      }
      lds_wait<1>(&s.cnt[CNT_V], 5);
      {
        const int dim = 64 * w + 2 * (lane & 31);
        const f32x4 f = *(const f32x4*)s.sf;
        const f32x2 o = (*(const f32x2*)&s.so[0][dim] * f[0] + *(const f32x2*)&s.so[1][dim] * f[1]) + (*(const f32x2*)&s.so[2][dim] * f[2] + *(const f32x2*)&s.so[3][dim] * f[3]);
        st_sc(rw, lane < 32 ? lane * 16 : OOB, WS_B + id * 2048 + w * 512, o[0], o[1], tag);
        u32x4 v[2];
        poll2(v, rw, (c8 >> 2) * 2048 + (8 * (c8 & 3) + r8) * 16, WS_B + bh * 8192 + w * 512, 4096, tag);
        float oa = __uint_as_float(v[0][0]) + __uint_as_float(v[1][0]), ob = __uint_as_float(v[0][2]) + __uint_as_float(v[1][2]);
        oa += dpp<0x104>(oa); ob += dpp<0x104>(ob);
        const int opk = cvtpk(oa, ob);
        float acc[NL][8] = {};
        static_for<4>([&](auto P) __attribute__((always_inline)) {
          constexpr int p = P.value;
          int op = __builtin_amdgcn_update_dpp(0, opk, 0x150 + p, 0xf, 0x3, false);
          op = __builtin_amdgcn_update_dpp(op, opk, 0x158 + p, 0xf, 0xc, false);
#pragma unroll
          for (int j = 0; j < NL; ++j)
#pragma unroll
            for (int e = 0; e < 4; ++e) { acc[j][2 * e] = dot2(wv[j][2 * p][e], op, acc[j][2 * e]); acc[j][2 * e + 1] = dot2(wv[j][2 * p + 1][e], op, acc[j][2 * e + 1]); }
        });
#pragma unroll
        for (int j = 0; j < NL; ++j) {
          float u[4];
#pragma unroll
          for (int e = 0; e < 4; ++e) u[e] = swap32_add(acc[j][e], acc[j][e + 4]);
          s.sy[w][64 * j + 8 * c8 + r8] = comb<XOR8>(swap16_add(u[0], u[2]), swap16_add(u[1], u[3]), bit3);
        }
      }
      FENCE();
      asm volatile("s_waitcnt lgkmcnt(0)" ::: "memory");
      int arrived = 0;
      if (lane == 0) arrived = __hip_atomic_fetch_add(&s.cnt[CNT_H], 1, __ATOMIC_RELAXED, __HIP_MEMORY_SCOPE_WORKGROUP);
      arrived = __builtin_amdgcn_readfirstlane(arrived);
      FENCE();
      if (arrived == 3) {
#pragma unroll
        for (int t2 = 0; t2 < (NL + 1) / 2; ++t2) {
          const bool on = lane < 32 * (NL - 2 * t2);
          const int col = 128 * t2 + 2 * (on ? lane : 0);
          const f32x2 y = (*(const f32x2*)&s.sy[0][col] + *(const f32x2*)&s.sy[1][col]) + (*(const f32x2*)&s.sy[2][col] + *(const f32x2*)&s.sy[3][col]);
          st_sc(rw, on ? r8 * 2048 + c8 * 16 : OOB, WS_C + (b * 28 + 4 * L0 + 8 * t2) * 2048 + h * 128, y[0], y[1], tag);
        }
      }
      if (S < 2 && w == 3 && reducer) {
        u32x4 v[2];
        poll2(v, rw, lane * 16, WS_C + (b * 28 + rc) * 2048, 1024, tag);
        const float a0 = rbf(hi3_sum(__uint_as_float(v[0][0]) + __uint_as_float(v[1][0]))), a1 = rbf(hi3_sum(__uint_as_float(v[0][2]) + __uint_as_float(v[1][2])));
        const float r0 = rbf(lo_f(xw) + a0) + y0, r1 = rbf(hi_f(xw) + a1) + y1;
        __builtin_amdgcn_raw_buffer_store_b32(cvtpk(r0, r1), mk_rsrc(a.out, 4 * D * 2), lane < 8 ? lane * 4 : OOB, (b * D + 16 * rc) * 2, 0);
      }
    };
    if (sidx == 0) post(IC<0>{}); else if (sidx == 1) post(IC<1>{}); else if (sidx == 2) post(IC<2>{}); else post(IC<3>{});
  }
}
}

extern "C" void kernel_launch(void* const* d_in, const int* in_sizes, int n_in, void* d_out, int out_size, void* d_ws,
                              size_t ws_size, hipStream_t stream) {
  Args a;
  a.x = (const unsigned short*)d_in[0]; a.K = (const unsigned short*)d_in[1]; a.V = (const unsigned short*)d_in[2];
  a.wq = (const unsigned short*)d_in[3]; a.wo = (const unsigned short*)d_in[4];
  a.wg = (const unsigned short*)d_in[6]; a.wu = (const unsigned short*)d_in[7]; a.wd = (const unsigned short*)d_in[8];
  a.out = (unsigned*)d_out; a.ws = (char*)d_ws;
  k_main<<<256, 512, 0, stream>>>(a);
}
```
